```python
import torch

def _softmax(x, axis=-1):
    return torch.softmax(x, dim=axis)
def _gelu(x):
    return torch.nn.functional.gelu(x, approximate="tanh")
def _silu(x):
    return torch.nn.functional.silu(x)
def _sigmoid(x):
    return torch.sigmoid(x)
def _layernorm(x, axis=-1, eps=1e-5):
    m = x.mean(dim=axis, keepdim=True)
    v = x.var(dim=axis, keepdim=True, unbiased=False)
    return (x - m) / torch.sqrt(v + eps)
def _scatter_add(tgt, idx, src, axis=0):
    return tgt.clone().index_add_(axis, idx, src)
def _segment_sum(src, seg, n):
    out = torch.zeros((n,) + src.shape[1:], dtype=src.dtype)
    return out.index_add_(0, seg, src)
def _pad(x, pw):
    flat = tuple(v for pair in reversed(pw) for v in pair)
    return torch.nn.functional.pad(x, flat)

kernel_name = 'recurrent_scan__mha_block_cached__swiglu_ffn__decode'

def setup_inputs():

    _w = (lambda a: (lambda t: t.to(torch.bfloat16) if t.is_floating_point() else t)(torch.as_tensor(a)))
    def _A_setup():
        B, Sq, D = 4, 1, 512
        H, Sk, Dh = 32, 8192, 128
        return {
            "x": _w((lambda *s: torch.randn(*s, dtype=torch.bfloat16) * 0.05)(B, Sq, D)),
            "k_cache": _w((lambda *s: torch.randn(*s, dtype=torch.bfloat16) * 1.6)(B, H, Sk, Dh)),
            "v_cache": _w((lambda *s: torch.randn(*s, dtype=torch.bfloat16) * 0.2)(B, H, Sk, Dh)),
            "wq": _w((lambda *s: torch.randn(*s, dtype=torch.bfloat16) * 1.6)(D, H * Dh) * (D ** -0.5)),
            "wo": _w((lambda *s: torch.randn(*s, dtype=torch.bfloat16) * 0.2)(H * Dh, D) * ((H * Dh) ** -0.5)),
        }
    _A_in = _A_setup()

    def _B_setup():
        B, S, D, F = 4, 1, 512, 1792
        return {
            "x": _w((lambda *s: torch.randn(*s, dtype=torch.bfloat16) * 0.05)(B, S, D)),
            "w_gate": _w((lambda *s: torch.randn(*s, dtype=torch.bfloat16) * 0.1)(D, F)),
            "w_up": _w((lambda *s: torch.randn(*s, dtype=torch.bfloat16) * 0.1)(D, F)),
            "w_down": _w((lambda *s: torch.randn(*s, dtype=torch.bfloat16) * 0.1)(F, D)),
        }
    _B_in = _B_setup()

    return {
        "A_x": _A_in["x"],
        "A_k_cache": _A_in["k_cache"],
        "A_v_cache": _A_in["v_cache"],
        "A_wq": _A_in["wq"],
        "A_wo": _A_in["wo"],
        "B_x": _B_in["x"],
        "B_w_gate": _B_in["w_gate"],
        "B_w_up": _B_in["w_up"],
        "B_w_down": _B_in["w_down"],
    }

def reference(A_x, A_k_cache, A_v_cache, A_wq, A_wo, B_x, B_w_gate, B_w_up, B_w_down):
    def _A_ref(x, k_cache, v_cache, wq, wo):
        B, Sq, D = x.shape
        H, Dh = 32, 128
        q = (lambda x, *d: x.permute(*d))((x @ wq).reshape(B, Sq, H, Dh), 0, 2, 1, 3)
        scores = torch.einsum("bhqd,bhkd->bhqk", q, k_cache) * (Dh ** -0.5)
        p = _softmax(scores, axis=-1)
        o = torch.einsum("bhqk,bhkd->bhqd", p, v_cache)
        return (lambda x, *d: x.permute(*d))(o, 0, 2, 1, 3).reshape(B, Sq, H * Dh) @ wo

    def _B_ref(x, w_gate, w_up, w_down):
        g = _silu(x @ w_gate)
        u = x @ w_up
        return (g * u) @ w_down

    x = A_x
    xn = _layernorm(x, axis=-1)
    s = _A_ref(xn, A_k_cache, A_v_cache, A_wq, A_wo)
    g = _silu(_B_ref(xn, B_w_gate, B_w_up, B_w_down))
    return x + s * g
```

```cpp
#include <hip/hip_runtime.h>
#include <hip/hip_bf16.h>
#include <cstdint>

typedef unsigned u32x4 __attribute__((ext_vector_type(4)));
typedef float f32x4 __attribute__((ext_vector_type(4)));
typedef __bf16 bf16x2 __attribute__((ext_vector_type(2)));

constexpr int B = 4, D = 512, H = 32, SK = 8192, DH = 128, F = 1792;
constexpr int NSPLIT = 2;

__device__ __forceinline__ float bf2f(unsigned short v) { return __uint_as_float((unsigned)v << 16); }
__device__ __forceinline__ float rbf(float x) { __bf16 b = (__bf16)x; return (float)b; }
__device__ __forceinline__ unsigned short f2bf(float x) { __bf16 b = (__bf16)x; return __builtin_bit_cast(unsigned short, b); }
__device__ __forceinline__ float lo16(unsigned v) { return __uint_as_float(v << 16); }
__device__ __forceinline__ float hi16(unsigned v) { return __uint_as_float(v & 0xffff0000u); }
__device__ __forceinline__ float dot2(unsigned k, unsigned q, float acc) {
  return __builtin_amdgcn_fdot2_f32_bf16(__builtin_bit_cast(bf16x2, k), __builtin_bit_cast(bf16x2, q), acc, false);
}
template <int CTRL> __device__ __forceinline__ float dpp(float x) {
  return __builtin_bit_cast(float, __builtin_amdgcn_mov_dpp(__builtin_bit_cast(int, x), CTRL, 0xf, 0xf, true));
}
constexpr int XOR1 = 0xB1, XOR2 = 0x4E, XOR7 = 0x141, XOR8 = 0x128;
__device__ __forceinline__ float row_sum16(float x) {
  x += dpp<XOR1>(x); x += dpp<XOR2>(x); x += dpp<XOR7>(x); x += dpp<XOR8>(x); return x;
}
__device__ __forceinline__ float wave_sum(float x) {
  for (int o = 32; o > 0; o >>= 1) x += __shfl_xor(x, o, 64);
  return x;
}

__device__ void layernorm_row(const unsigned short* x, float* xn, float* red, int tid, int nthr) {
  float s = 0.f;
  for (int i = tid; i < D; i += nthr) s += bf2f(x[i]);
  s = wave_sum(s);
  if ((tid & 63) == 0) red[tid >> 6] = s;
  __syncthreads();
  float tot = 0.f;
  for (int w = 0; w < nthr / 64; ++w) tot += red[w];
  const float meanf = tot * (1.f / D);
  __syncthreads();
  float v = 0.f;
  for (int i = tid; i < D; i += nthr) { float d = bf2f(x[i]) - meanf; v += d * d; }
  v = wave_sum(v);
  if ((tid & 63) == 0) red[tid >> 6] = v;
  __syncthreads();
  float vt = 0.f;
  for (int w = 0; w < nthr / 64; ++w) vt += red[w];
  const float var = rbf(vt * (1.f / D));
  const float mean = rbf(meanf);
  const float den = rbf(sqrtf(rbf(var + 1e-5f)));
  for (int i = tid; i < D; i += nthr) xn[i] = rbf(rbf(bf2f(x[i]) - mean) / den);
  __syncthreads();
}

__global__ void __launch_bounds__(256) k_prep(const unsigned short* x, const unsigned short* wq, const unsigned short* wg, const unsigned short* wu,
                                               unsigned short* q, float* hbuf) {
  __shared__ float xn[D]; __shared__ float red[8];
  const int b = blockIdx.x, j = blockIdx.y, tid = threadIdx.x;
  layernorm_row(x + b * D, xn, red, tid, 256);
  if (j < 16) {
    const int n = j * 256 + tid;
    float a = 0.f;
    for (int k = 0; k < D; ++k) a = fmaf(xn[k], bf2f(wq[(size_t)k * (H * DH) + n]), a);
    q[b * H * DH + n] = f2bf(a);
  } else {
    const int n = (j - 16) * 256 + tid;
    float g = 0.f, u = 0.f;
    for (int k = 0; k < D; ++k) { g = fmaf(xn[k], bf2f(wg[(size_t)k * F + n]), g); u = fmaf(xn[k], bf2f(wu[(size_t)k * F + n]), u); }
    g = rbf(g); u = rbf(u);
    const float sg = rbf(g / (1.f + __expf(-g)));
    hbuf[b * F + n] = rbf(sg * u);
  }
}

constexpr int NW = 8;
__global__ void __launch_bounds__(512) k_attn(const unsigned short* __restrict__ q, const unsigned short* __restrict__ kc, const unsigned short* __restrict__ vc,
                                               float* pml, float* po) {
  __shared__ float lds[NW * 4 * (DH + 4)];
  const int bh = blockIdx.x >> 1, sp = blockIdx.x & 1;
  const int tid = threadIdx.x, lane = tid & 63, wave = __builtin_amdgcn_readfirstlane(tid >> 6), r = lane >> 4, c = lane & 15;
  constexpr float C2 = 0.088388347648318440f * 1.4426950408889634f;
  const u32x4 qv = *reinterpret_cast<const u32x4*>(q + bh * DH + 8 * c);
  const size_t base = ((size_t)bh * SK + sp * (SK / NSPLIT)) * DH;
  const u32x4* kp = reinterpret_cast<const u32x4*>(kc + base) + lane;
  const u32x4* vp = reinterpret_cast<const u32x4*>(vc + base) + lane;
  constexpr int NIT = SK / NSPLIT / 16 / NW;
  float m = -INFINITY, l = 0.f, acc[8];
#pragma unroll
  for (int k = 0; k < 8; ++k) acc[k] = 0.f;
  u32x4 Kn[4], Vn[4];
#define ISSUE(i_) do { const int o_ = ((i_) * NW + wave) * 256; _Pragma("unroll") for (int t = 0; t < 4; ++t) Kn[t] = __builtin_nontemporal_load(kp + o_ + t * 64); \
    _Pragma("unroll") for (int t = 0; t < 4; ++t) Vn[t] = __builtin_nontemporal_load(vp + o_ + t * 64); } while (0)
  ISSUE(0);
  for (int i = 0; i < NIT; ++i) {
    u32x4 Kc[4], Vc[4];
#pragma unroll
    for (int t = 0; t < 4; ++t) { Kc[t] = Kn[t]; Vc[t] = Vn[t]; }
    if (i + 1 < NIT) ISSUE(i + 1);
    float s[4];
#pragma unroll
    for (int t = 0; t < 4; ++t) { float d = 0.f;
#pragma unroll
      for (int k = 0; k < 4; ++k) d = dot2(Kc[t][k], qv[k], d);
      s[t] = rbf(rbf(row_sum16(d)) * 0.088388347648318440f) * 1.4426950408889634f; }
    const float mn = fmaxf(fmaxf(fmaxf(s[0], s[1]), fmaxf(s[2], s[3])), m);
    const float alpha = __builtin_amdgcn_exp2f(m - mn);
    m = mn;
    float p[4];
#pragma unroll
    for (int t = 0; t < 4; ++t) p[t] = __builtin_amdgcn_exp2f(s[t] - mn);
    l = fmaf(l, alpha, (p[0] + p[1]) + (p[2] + p[3]));
#pragma unroll
    for (int k = 0; k < 8; ++k) acc[k] *= alpha;
#pragma unroll
    for (int t = 0; t < 4; ++t)
#pragma unroll
      for (int k = 0; k < 4; ++k) { acc[2 * k] = fmaf(p[t], lo16(Vc[t][k]), acc[2 * k]); acc[2 * k + 1] = fmaf(p[t], hi16(Vc[t][k]), acc[2 * k + 1]); }
  }
  float* rec = lds + (wave * 4 + r) * (DH + 4);
#pragma unroll
  for (int k = 0; k < 8; ++k) rec[4 + 8 * c + k] = acc[k];
  if (c == 0) { rec[0] = m; rec[1] = l; }
  __syncthreads();
  if (tid < DH) {
    float M = -INFINITY;
    for (int w = 0; w < NW * 4; ++w) M = fmaxf(M, lds[w * (DH + 4)]);
    float L = 0.f, o = 0.f;
    for (int w = 0; w < NW * 4; ++w) { const float wt = __builtin_amdgcn_exp2f(lds[w * (DH + 4)] - M); L = fmaf(wt, lds[w * (DH + 4) + 1], L); o = fmaf(wt, lds[w * (DH + 4) + 4 + tid], o); }
    po[blockIdx.x * DH + tid] = o;
    if (tid == 0) { pml[2 * blockIdx.x] = M; pml[2 * blockIdx.x + 1] = L; }
  }
}

__global__ void __launch_bounds__(256) k_final(const unsigned short* x, const unsigned short* wo, const unsigned short* wd, const float* pml, const float* po, const float* hbuf,
                                                unsigned short* out) {
  __shared__ float o[H * DH]; __shared__ float red[2][4][64];
  const int b = blockIdx.x, tid = threadIdx.x;
  for (int i = tid; i < H * DH; i += 256) {
    const int h = i / DH, d = i % DH, w0 = (b * H + h) * 2;
    const float m0 = pml[2 * w0], l0 = pml[2 * w0 + 1], m1 = pml[2 * w0 + 2], l1 = pml[2 * w0 + 3];
    const float M = fmaxf(m0, m1), a0 = __builtin_amdgcn_exp2f(m0 - M), a1 = __builtin_amdgcn_exp2f(m1 - M);
    o[i] = rbf((a0 * po[w0 * DH + d] + a1 * po[(w0 + 1) * DH + d]) / (a0 * l0 + a1 * l1));
  }
  __syncthreads();
  const int n = blockIdx.y * 64 + (tid & 63), g = tid >> 6;
  float s = 0.f, f = 0.f;
  for (int k = g; k < H * DH; k += 4) s = fmaf(o[k], bf2f(wo[(size_t)k * D + n]), s);
  for (int k = g; k < F; k += 4) f = fmaf(hbuf[b * F + k], bf2f(wd[(size_t)k * D + n]), f);
  red[0][g][tid & 63] = s; red[1][g][tid & 63] = f;
  __syncthreads();
  if (tid < 64) {
    s = rbf(red[0][0][tid] + red[0][1][tid] + red[0][2][tid] + red[0][3][tid]);
    f = rbf(red[1][0][tid] + red[1][1][tid] + red[1][2][tid] + red[1][3][tid]);
    const float gg = rbf(f / (1.f + __expf(-f)));
    out[b * D + n] = f2bf(bf2f(x[b * D + n]) + rbf(s * gg));
  }
}

extern "C" void kernel_launch(void* const* d_in, const int* in_sizes, int n_in, void* d_out, int out_size, void* d_ws, size_t ws_size, hipStream_t stream) {
  const unsigned short* x = (const unsigned short*)d_in[0];
  const unsigned short* kc = (const unsigned short*)d_in[1];
  const unsigned short* vc = (const unsigned short*)d_in[2];
  const unsigned short* wq = (const unsigned short*)d_in[3];
  const unsigned short* wo = (const unsigned short*)d_in[4];
  const unsigned short* wg = (const unsigned short*)d_in[6];
  const unsigned short* wu = (const unsigned short*)d_in[7];
  const unsigned short* wd = (const unsigned short*)d_in[8];
  char* ws = (char*)d_ws;
  unsigned short* q = (unsigned short*)ws;
  float* hbuf = (float*)(ws + 65536);
  float* pml = (float*)(ws + 131072);
  float* po = (float*)(ws + 196608);
  k_prep<<<dim3(B, 16 + 7), 256, 0, stream>>>(x, wq, wg, wu, q, hbuf);
  k_attn<<<256, 512, 0, stream>>>(q, kc, vc, pml, po);
  k_final<<<dim3(B, 8), 256, 0, stream>>>(x, wo, wd, pml, po, hbuf, (unsigned short*)d_out);
}
```

```cpp
#include <hip/hip_runtime.h>
#include <hip/hip_bf16.h>
#include <cstdint>

typedef unsigned u32x4 __attribute__((ext_vector_type(4)));
typedef float f32x4 __attribute__((ext_vector_type(4)));
typedef __bf16 bf16x2 __attribute__((ext_vector_type(2)));
typedef unsigned long long u64;
#define GAS __attribute__((address_space(1)))
typedef GAS unsigned gu32;
typedef GAS u64 gu64;
#define RLX_AGENT __ATOMIC_RELAXED, __HIP_MEMORY_SCOPE_AGENT

constexpr int B = 4, D = 512, H = 32, SK = 8192, DH = 128, F = 1792;
constexpr int NWG = 256, NW = 8, NT = NW * 64;
constexpr int KEYS_WG = SK / 2;
constexpr int NIT = KEYS_WG / 16 / NW;
#define AUX_KV 2
#define AUX_W 2
#define POLL_T 8
#define STAMPS 0
constexpr size_t WS_EPOCH = 0;
constexpr size_t WS_ML = 4096;
constexpr size_t WS_H = WS_ML + (size_t)NWG * NW * 16;
constexpr size_t WS_Y = WS_H + (size_t)B * (F / 2) * 8;
constexpr size_t WS_END = WS_Y + (size_t)NWG * D * 8;
constexpr size_t WS_STAMP = 4u << 20;
constexpr unsigned SPIN_CAP = 1u << 20;

__device__ __forceinline__ float bf2f(unsigned v) { return __uint_as_float(v << 16); }
__device__ __forceinline__ float rbf(float x) { __bf16 b = (__bf16)x; return (float)b; }
__device__ __forceinline__ unsigned f2bf(float x) { __bf16 b = (__bf16)x; return __builtin_bit_cast(unsigned short, b); }
__device__ __forceinline__ float lo16(unsigned v) { return __uint_as_float(v << 16); }
__device__ __forceinline__ float hi16(unsigned v) { return __uint_as_float(v & 0xffff0000u); }
__device__ __forceinline__ float dot2(unsigned k, unsigned q, float acc) {
  return __builtin_amdgcn_fdot2_f32_bf16(__builtin_bit_cast(bf16x2, k), __builtin_bit_cast(bf16x2, q), acc, false);
}
template <int CTRL> __device__ __forceinline__ float dpp(float x) {
  return __builtin_bit_cast(float, __builtin_amdgcn_mov_dpp(__builtin_bit_cast(int, x), CTRL, 0xf, 0xf, true));
}
constexpr int XOR1 = 0xB1, XOR2 = 0x4E, XOR7 = 0x141, XOR8 = 0x128;
__device__ __forceinline__ float row_sum16(float x) { x += dpp<XOR1>(x); x += dpp<XOR2>(x); x += dpp<XOR7>(x); x += dpp<XOR8>(x); return x; }
__device__ __forceinline__ float tr32(float a, float b) {
  auto s = __builtin_amdgcn_permlane32_swap(__float_as_uint(a), __float_as_uint(b), false, false);
  return __uint_as_float(s[0]) + __uint_as_float(s[1]);
}
__device__ __forceinline__ float tr16(float a, float b) {
  auto s = __builtin_amdgcn_permlane16_swap(__float_as_uint(a), __float_as_uint(b), false, false);
  return __uint_as_float(s[0]) + __uint_as_float(s[1]);
}
template <int CTRL> __device__ __forceinline__ float trd(float a, float b, bool hi) {
  const float keep = hi ? b : a, send = hi ? a : b;
  return keep + dpp<CTRL>(send);
}
__device__ __forceinline__ float xrow_sum(float x) { x = tr16(x, x); return tr32(x, x); }
__device__ __forceinline__ float xrow_max(float x) {
  auto s = __builtin_amdgcn_permlane16_swap(__float_as_uint(x), __float_as_uint(x), false, false);
  x = fmaxf(__uint_as_float(s[0]), __uint_as_float(s[1]));
  auto t = __builtin_amdgcn_permlane32_swap(__float_as_uint(x), __float_as_uint(x), false, false);
  return fmaxf(__uint_as_float(t[0]), __uint_as_float(t[1]));
}
__device__ __forceinline__ float wave_sum(float x) { return xrow_sum(row_sum16(x)); }
__device__ __forceinline__ float reduce8_all(const float (&v)[8], int lane) {
  const float a0 = tr32(v[0], v[4]), a1 = tr32(v[1], v[5]), a2 = tr32(v[2], v[6]), a3 = tr32(v[3], v[7]);
  const float b0 = tr16(a0, a2), b1 = tr16(a1, a3);
  float c = trd<XOR8>(b0, b1, lane & 8);
  c += dpp<XOR7>(c); c += dpp<XOR2>(c); c += dpp<XOR1>(c);
  return c;
}
__device__ __forceinline__ float silu_f(float x) { return x / (1.f + __expf(-x)); }
__device__ __forceinline__ u64 granule(unsigned value, unsigned tag) { return ((u64)tag << 32) | value; }
#define RSRC(p, n) __builtin_amdgcn_make_buffer_rsrc((void*)(p), 0, (int)(n), 0x00020000)
#define BLOAD(r, vo, so, aux) __builtin_amdgcn_raw_buffer_load_b128((r), (vo), (so), (aux))
#define STAMP(i) do {} while (0)

constexpr int L_XS = 0;
constexpr int L_QP = L_XS + NW * 64;
constexpr int L_FP = L_QP + NW * DH;
constexpr int L_REC = L_FP + NW * 64;
constexpr int L_ML = L_REC + NW * DH;
constexpr int L_OT = L_ML + 16;
constexpr int L_F2 = L_OT + NW * DH;
constexpr int L_CNT = L_F2 + NW * 8;
constexpr int L_END = L_CNT + 4;

__global__ void __launch_bounds__(NT, 2) fused_decode(const unsigned short* __restrict__ x, const unsigned short* __restrict__ kc, const unsigned short* __restrict__ vc,
                                                      const unsigned short* __restrict__ wq, const unsigned short* __restrict__ wo, const unsigned short* __restrict__ wg,
                                                      const unsigned short* __restrict__ wu, const unsigned short* __restrict__ wd, unsigned short* __restrict__ out,
                                                      unsigned char* __restrict__ ws) {
  __shared__ __attribute__((aligned(16))) float lds[L_END];
  const int tid = threadIdx.x, lane = tid & 63, wave = __builtin_amdgcn_readfirstlane(tid >> 6);
  const int r = lane >> 4, c = lane & 15;
  const int x8 = blockIdx.x & 7, i32 = blockIdx.x >> 3;
  const int h = 4 * x8 + (i32 >> 3), b = (i32 >> 1) & 3, sp = i32 & 1;
  const int slot = b * 64 + 2 * h + sp;
  const int cb = 8 * x8 + (i32 & 7), bc = i32 >> 3;
  const int j1 = 28 * x8 + i32;
  STAMP(0);

  const unsigned epoch = __hip_atomic_load((gu32*)(ws + WS_EPOCH), RLX_AGENT);
  const auto rx = RSRC(x, B * D * 2);
  u32x4 xv[4]; unsigned xk[4];
#pragma unroll
  for (int t = 0; t < 4; ++t) xv[t] = BLOAD(rx, lane * 16, t * 1024, 0);
  __builtin_amdgcn_sched_barrier(0);
#pragma unroll
  for (int t = 0; t < 4; ++t) xk[t] = __builtin_amdgcn_raw_buffer_load_b16(rx, tid * 2, t * 1024, 0);
  __builtin_amdgcn_sched_barrier(0);
  const auto rwq = RSRC(wq + h * DH, D * H * DH * 2 - h * DH * 2);
  u32x4 Wq[16];
#pragma unroll
  for (int i = 0; i < 16; ++i) Wq[i] = BLOAD(rwq, (16 * r) * 8192 + c * 16, (64 * wave + i) * 8192, AUX_W);
  __builtin_amdgcn_sched_barrier(0);
  const size_t kvbase = ((size_t)(b * H + h) * SK + (size_t)sp * KEYS_WG) * DH;
  const auto rk = RSRC(kc + kvbase, KEYS_WG * DH * 2);
  const auto rv = RSRC(vc + kvbase, KEYS_WG * DH * 2);
  u32x4 K[4], V[4];
#define ISSUE_K(it) do { const unsigned so_ = ((it) * NW + wave) * 4096; _Pragma("unroll") for (int t = 0; t < 4; ++t) K[t] = BLOAD(rk, lane * 16 + t * 1024, so_, AUX_KV); } while (0)
#define ISSUE_V(it) do { const unsigned so_ = ((it) * NW + wave) * 4096; _Pragma("unroll") for (int t = 0; t < 4; ++t) V[t] = BLOAD(rv, lane * 16 + t * 1024, so_, AUX_KV); } while (0)
  ISSUE_K(0);
  __builtin_amdgcn_sched_barrier(0);
  ISSUE_V(0);
  __builtin_amdgcn_sched_barrier(0);
  const int ffn1_bytes = i32 < 28 ? D * F * 2 : 0;
  const u32x4 Wg = BLOAD(RSRC(wg, ffn1_bytes), tid * (F * 2), j1 * 16, AUX_W);
  const u32x4 Wu = BLOAD(RSRC(wu, ffn1_bytes), tid * (F * 2), j1 * 16, AUX_W);
  __builtin_amdgcn_sched_barrier(0);
  const auto rwo = RSRC(wo + (size_t)h * DH * D, DH * D * 2);
  u32x4 Wo[16];
#pragma unroll
  for (int i = 0; i < 16; ++i) Wo[i] = BLOAD(rwo, (lane >> 3) * 1024 + (lane & 7) * 16, i * 8192 + wave * 128, AUX_W);
  __builtin_amdgcn_sched_barrier(0);
  const auto rwd = RSRC(wd, F * D * 2);
  u32x4 Wd[4];
#pragma unroll
  for (int t = 0; t < 4; ++t) Wd[t] = BLOAD(rwd, (lane < 56 ? lane * 4096 : 0x40000000) + t * 1024, wave * 224 * 1024 + cb * 16, AUX_W);
  if (tid == 0) reinterpret_cast<unsigned*>(lds)[L_CNT] = 0u;
  asm volatile("" ::: "memory");
  __builtin_amdgcn_sched_barrier(0);

  float xn[4];
#pragma unroll
  for (int t = 0; t < 4; ++t) {
    float e[8];
#pragma unroll
    for (int k = 0; k < 4; ++k) { e[2 * k] = lo16(xv[t][k]); e[2 * k + 1] = hi16(xv[t][k]); }
    const float mean = wave_sum(((e[0] + e[1]) + (e[2] + e[3])) + ((e[4] + e[5]) + (e[6] + e[7]))) * (1.f / D);
    float q2 = 0.f;
#pragma unroll
    for (int k = 0; k < 8; ++k) { const float d = e[k] - mean; q2 = fmaf(d, d, q2); }
    const float var = rbf(wave_sum(q2) * (1.f / D));
    const float den = rbf(sqrtf(rbf(var + 1e-5f)));
    xn[t] = rbf(rbf(bf2f(xk[t]) - rbf(mean)) / den);
  }
  lds[L_XS + wave * 64 + lane] = b == 0 ? xn[0] : b == 1 ? xn[1] : b == 2 ? xn[2] : xn[3];
  STAMP(1);
  {
    float xr[16];
#pragma unroll
    for (int u = 0; u < 4; ++u) { const f32x4 t4 = *reinterpret_cast<const f32x4*>(lds + L_XS + wave * 64 + 16 * r + 4 * u); xr[4 * u] = t4[0]; xr[4 * u + 1] = t4[1]; xr[4 * u + 2] = t4[2]; xr[4 * u + 3] = t4[3]; }
    float a[8];
#pragma unroll
    for (int k = 0; k < 8; ++k) a[k] = 0.f;
#pragma unroll
    for (int i = 0; i < 16; ++i)
#pragma unroll
      for (int k = 0; k < 4; ++k) { a[2 * k] = fmaf(xr[i], lo16(Wq[i][k]), a[2 * k]); a[2 * k + 1] = fmaf(xr[i], hi16(Wq[i][k]), a[2 * k + 1]); }
    const float s0 = tr32(a[0], a[4]), s1 = tr32(a[1], a[5]), s2 = tr32(a[2], a[6]), s3 = tr32(a[3], a[7]);
    const float t0 = tr16(s0, s2), t1 = tr16(s1, s3);
    *reinterpret_cast<float2*>(lds + L_QP + wave * DH + 8 * c + 2 * r) = make_float2(t0, t1);
  }
  __syncthreads();
  u32x4 qv;
  {
    f32x4 s0 = {0.f, 0.f, 0.f, 0.f}, s1 = {0.f, 0.f, 0.f, 0.f};
#pragma unroll
    for (int w = 0; w < NW; ++w) { s0 += *reinterpret_cast<const f32x4*>(lds + L_QP + w * DH + 8 * c); s1 += *reinterpret_cast<const f32x4*>(lds + L_QP + w * DH + 8 * c + 4); }
    qv[0] = f2bf(s0[0]) | (f2bf(s0[1]) << 16); qv[1] = f2bf(s0[2]) | (f2bf(s0[3]) << 16);
    qv[2] = f2bf(s1[0]) | (f2bf(s1[1]) << 16); qv[3] = f2bf(s1[2]) | (f2bf(s1[3]) << 16);
  }
  unsigned tag = __builtin_amdgcn_readfirstlane(epoch) + 1u;
  STAMP(2);

  constexpr float SCALE = 0.088388347648318440f, LOG2E = 1.4426950408889634f;
  float m = -INFINITY, l = 0.f, acc[8];
#pragma unroll
  for (int k = 0; k < 8; ++k) acc[k] = 0.f;
#define BODY(it) do { \
    float s_[4]; \
    _Pragma("unroll") for (int t = 0; t < 4; ++t) { float d_ = dot2(K[t][0], qv[0], 0.f); d_ = dot2(K[t][1], qv[1], d_); d_ = dot2(K[t][2], qv[2], d_); s_[t] = dot2(K[t][3], qv[3], d_); } \
    asm volatile("" : "+v"(s_[0]), "+v"(s_[1]), "+v"(s_[2]), "+v"(s_[3])); \
    __builtin_amdgcn_sched_barrier(0); \
    ISSUE_K((it) + 1); \
    __builtin_amdgcn_sched_barrier(0); \
    _Pragma("unroll") for (int t = 0; t < 4; ++t) s_[t] = rbf(rbf(row_sum16(s_[t])) * SCALE) * LOG2E; \
    const float mn_ = fmaxf(fmaxf(fmaxf(s_[0], s_[1]), fmaxf(s_[2], s_[3])), m); \
    const float al_ = __builtin_amdgcn_exp2f(m - mn_); \
    m = mn_; \
    _Pragma("unroll") for (int t = 0; t < 4; ++t) s_[t] = __builtin_amdgcn_exp2f(s_[t] - mn_); \
    l = fmaf(l, al_, (s_[0] + s_[1]) + (s_[2] + s_[3])); \
    _Pragma("unroll") for (int k = 0; k < 8; ++k) acc[k] *= al_; \
    _Pragma("unroll") for (int t = 0; t < 4; ++t) \
      _Pragma("unroll") for (int k = 0; k < 4; ++k) { acc[2 * k] = fmaf(s_[t], lo16(V[t][k]), acc[2 * k]); acc[2 * k + 1] = fmaf(s_[t], hi16(V[t][k]), acc[2 * k + 1]); } \
    asm volatile("" : "+v"(acc[0]), "+v"(acc[1]), "+v"(acc[2]), "+v"(acc[3]), "+v"(acc[4]), "+v"(acc[5]), "+v"(acc[6]), "+v"(acc[7])); \
    __builtin_amdgcn_sched_barrier(0); \
    ISSUE_V((it) + 1); \
    __builtin_amdgcn_sched_barrier(0); \
  } while (0)
  BODY(0);

  {
    float wv[16];
#pragma unroll
    for (int k = 0; k < 4; ++k) { wv[2 * k] = lo16(Wg[k]); wv[2 * k + 1] = hi16(Wg[k]); wv[8 + 2 * k] = lo16(Wu[k]); wv[8 + 2 * k + 1] = hi16(Wu[k]); }
    float p16[16];
#pragma unroll
    for (int k = 0; k < 16; ++k) p16[k] = tr16(tr32(xn[0] * wv[k], xn[2] * wv[k]), tr32(xn[1] * wv[k], xn[3] * wv[k]));
    float p8[8], p4[4], p2[2];
#pragma unroll
    for (int k = 0; k < 8; ++k) p8[k] = trd<XOR8>(p16[k], p16[k + 8], lane & 8);
#pragma unroll
    for (int k = 0; k < 4; ++k) p4[k] = trd<XOR7>(p8[k], p8[k + 4], lane & 4);
#pragma unroll
    for (int k = 0; k < 2; ++k) p2[k] = trd<XOR2>(p4[k], p4[k + 2], lane & 2);
    lds[L_FP + wave * 64 + lane] = trd<XOR1>(p2[0], p2[1], lane & 1);
    unsigned arr = 0;
    __builtin_amdgcn_fence(__ATOMIC_RELEASE, "workgroup");
    if (lane == 0) arr = __hip_atomic_fetch_add(reinterpret_cast<unsigned*>(lds) + L_CNT, 1u, __ATOMIC_RELAXED, __HIP_MEMORY_SCOPE_WORKGROUP);
    arr = __builtin_amdgcn_readfirstlane(arr);
    if (arr == NW - 1 && i32 < 28) {
      __builtin_amdgcn_fence(__ATOMIC_ACQUIRE, "workgroup");
      float s = 0.f;
#pragma unroll
      for (int w = 0; w < NW; ++w) s += lds[L_FP + w * 64 + lane];
      s = rbf(s);
      const float u = dpp<XOR8>(s);
      const float hv = rbf(rbf(silu_f(s)) * u);
      const float hn = dpp<XOR1>(hv);
      if ((lane & 9) == 0)
        __hip_atomic_store((gu64*)(ws + WS_H) + (lane >> 4) * (F / 2) + j1 * 4 + ((lane & 7) >> 1), granule(f2bf(hv) | (f2bf(hn) << 16), tag), RLX_AGENT);
    }
  }
  STAMP(3);
  const int itA = 6 + 2 * wave;
  int it = 1;
  for (; it < itA; ++it) BODY(it);

  {
    gu64* hg = (gu64*)(ws + WS_H) + bc * (F / 2) + wave * 112 + (lane < 56 ? lane * 2 : 0);
    u64 g0, g1;
    for (unsigned spins = 0;; ++spins) {
      g0 = __hip_atomic_load(hg, RLX_AGENT); g1 = __hip_atomic_load(hg + 1, RLX_AGENT);
      const bool ok = (unsigned)(g0 >> 32) == tag && (unsigned)(g1 >> 32) == tag;
      if (__all(ok) || spins > SPIN_CAP) break;
      __builtin_amdgcn_s_sleep(8);
    }
    const float hv[4] = {lo16((unsigned)g0), hi16((unsigned)g0), lo16((unsigned)g1), hi16((unsigned)g1)};
    float a[8];
#pragma unroll
    for (int k = 0; k < 8; ++k) a[k] = 0.f;
#pragma unroll
    for (int t = 0; t < 4; ++t)
#pragma unroll
      for (int k = 0; k < 4; ++k) { a[2 * k] = fmaf(hv[t], lo16(Wd[t][k]), a[2 * k]); a[2 * k + 1] = fmaf(hv[t], hi16(Wd[t][k]), a[2 * k + 1]); }
    const float f = reduce8_all(a, lane);
    if ((lane & 7) == 0) lds[L_F2 + wave * 8 + (lane >> 3)] = f;
  }
  const unsigned xo = __builtin_amdgcn_raw_buffer_load_b16(rx, (lane >> 3) * 2, bc * 1024 + cb * 16, 0);
  STAMP(4);
  for (; it < NIT; ++it) BODY(it);
  STAMP(5);

  {
    const float mw = xrow_max(m), sc = __builtin_amdgcn_exp2f(m - mw);
    const float lw = xrow_sum(l * sc);
    const float s0 = tr32(acc[0] * sc, acc[4] * sc), s1 = tr32(acc[1] * sc, acc[5] * sc), s2 = tr32(acc[2] * sc, acc[6] * sc), s3 = tr32(acc[3] * sc, acc[7] * sc);
    *reinterpret_cast<float2*>(lds + L_REC + wave * DH + 8 * c + 2 * r) = make_float2(tr16(s0, s2), tr16(s1, s3));
    if (lane == 0) { lds[L_ML + wave] = mw; lds[L_ML + 8 + wave] = lw; }
  }
  __syncthreads();
  float M, L;
  {
    const f32x4 m0 = *reinterpret_cast<const f32x4*>(lds + L_ML), m1 = *reinterpret_cast<const f32x4*>(lds + L_ML + 4);
    const f32x4 l0 = *reinterpret_cast<const f32x4*>(lds + L_ML + 8), l1 = *reinterpret_cast<const f32x4*>(lds + L_ML + 12);
    M = fmaxf(fmaxf(fmaxf(m0[0], m0[1]), fmaxf(m0[2], m0[3])), fmaxf(fmaxf(m1[0], m1[1]), fmaxf(m1[2], m1[3])));
    float o0 = 0.f, o1 = 0.f; L = 0.f;
#pragma unroll
    for (int w = 0; w < NW; ++w) {
      const float sc = __builtin_amdgcn_exp2f((w < 4 ? m0[w & 3] : m1[w & 3]) - M);
      const float2 v = *reinterpret_cast<const float2*>(lds + L_REC + w * DH + 2 * lane);
      L = fmaf(sc, w < 4 ? l0[w & 3] : l1[w & 3], L); o0 = fmaf(sc, v.x, o0); o1 = fmaf(sc, v.y, o1);
    }
    float* ot = lds + L_OT + wave * DH;
    ot[((2 * lane) & 7) * 16 + (lane >> 2)] = o0;
    ot[(((2 * lane) & 7) + 1) * 16 + (lane >> 2)] = o1;
    float orow[16];
#pragma unroll
    for (int u = 0; u < 4; ++u) { const f32x4 t4 = *reinterpret_cast<const f32x4*>(ot + (lane >> 3) * 16 + 4 * u); orow[4 * u] = t4[0]; orow[4 * u + 1] = t4[1]; orow[4 * u + 2] = t4[2]; orow[4 * u + 3] = t4[3]; }
    float a[8];
#pragma unroll
    for (int k = 0; k < 8; ++k) a[k] = 0.f;
#pragma unroll
    for (int i = 0; i < 16; ++i)
#pragma unroll
      for (int k = 0; k < 4; ++k) { a[2 * k] = fmaf(orow[i], lo16(Wo[i][k]), a[2 * k]); a[2 * k + 1] = fmaf(orow[i], hi16(Wo[i][k]), a[2 * k + 1]); }
    const float a0 = tr32(a[0], a[4]), a1 = tr32(a[1], a[5]), a2 = tr32(a[2], a[6]), a3 = tr32(a[3], a[7]);
    const float y = trd<XOR8>(tr16(a0, a2), tr16(a1, a3), lane & 8);
    __hip_atomic_store((gu64*)(ws + WS_Y) + slot * D + 64 * wave + 8 * (lane & 7) + (lane >> 3), granule(__float_as_uint(y), tag), RLX_AGENT);
    if (lane < 2) __hip_atomic_store((gu64*)(ws + WS_ML) + (slot * NW + wave) * 2 + lane, granule(__float_as_uint(lane ? L : M), tag), RLX_AGENT);
  }
  STAMP(6);

  if (wave == 0) {
    float f = 0.f;
#pragma unroll
    for (int w = 0; w < NW; ++w) f += lds[L_F2 + w * 8 + (lane >> 3)];
    const float gate = rbf(silu_f(rbf(f)));
    gu64* mlp = (gu64*)(ws + WS_ML) + ((bc * 64 + lane) * NW + (cb >> 3)) * 2;
    gu64* yp = (gu64*)(ws + WS_Y) + (bc * 64 + lane) * D + 8 * cb;
    unsigned spins = 0;
    bool ok = false;
    for (;; ++spins) {
      if (!ok) ok = (unsigned)(__hip_atomic_load(mlp + 1, RLX_AGENT) >> 32) == tag;
      if (__popcll(__ballot(!ok)) <= POLL_T || spins > SPIN_CAP) break;
      __builtin_amdgcn_s_sleep(8);
    }
    u64 gm = 0, gl = 0, gy[8] = {0, 0, 0, 0, 0, 0, 0, 0};
    ok = false;
    for (;; ++spins) {
      if (!ok) {
        gm = __hip_atomic_load(mlp, RLX_AGENT); gl = __hip_atomic_load(mlp + 1, RLX_AGENT);
#pragma unroll
        for (int k = 0; k < 8; ++k) gy[k] = __hip_atomic_load(yp + k, RLX_AGENT);
        ok = (unsigned)(gm >> 32) == tag && (unsigned)(gl >> 32) == tag;
#pragma unroll
        for (int k = 0; k < 8; ++k) ok &= (unsigned)(gy[k] >> 32) == tag;
      }
      if (__all(ok) || spins > SPIN_CAP) break;
    }
    const float mp = __uint_as_float((unsigned)gm), lp = __uint_as_float((unsigned)gl);
    const float e = __builtin_amdgcn_exp2f(mp - fmaxf(mp, dpp<XOR1>(mp)));
    const float el = e * lp;
    const float wgt = e / (el + dpp<XOR1>(el));
    float a[8];
#pragma unroll
    for (int k = 0; k < 8; ++k) a[k] = wgt * __uint_as_float((unsigned)gy[k]);
    const float s = rbf(reduce8_all(a, lane));
    if ((lane & 7) == 0) out[bc * D + 8 * cb + (lane >> 3)] = (unsigned short)f2bf(bf2f(xo) + rbf(s * gate));
    STAMP(7);
  } else if (wave == 1 && blockIdx.x == 0) {
    gu64* mlp = (gu64*)(ws + WS_ML) + (lane * 4 * NW) * 2;
    for (unsigned spins = 0;; ++spins) {
      bool ok = true;
#pragma unroll
      for (int k = 0; k < 4; ++k) ok &= (unsigned)(__hip_atomic_load(mlp + k * NW * 2, RLX_AGENT) >> 32) == tag;
      if (__all(ok) || spins > SPIN_CAP) break;
      __builtin_amdgcn_s_sleep(2);
    }
    if (lane == 0) __hip_atomic_store((gu32*)(ws + WS_EPOCH), tag, RLX_AGENT);
  }
}

extern "C" void kernel_launch(void* const* d_in, const int* in_sizes, int n_in, void* d_out, int out_size, void* d_ws, size_t ws_size, hipStream_t stream) {
  fused_decode<<<NWG, NT, 0, stream>>>((const unsigned short*)d_in[0], (const unsigned short*)d_in[1], (const unsigned short*)d_in[2], (const unsigned short*)d_in[3],
                                       (const unsigned short*)d_in[4], (const unsigned short*)d_in[6], (const unsigned short*)d_in[7], (const unsigned short*)d_in[8],
                                       (unsigned short*)d_out, (unsigned char*)d_ws);
}
```

```cpp
#include <hip/hip_runtime.h>
#include <hip/hip_bf16.h>
#include <cstdint>
#include <cstdio>

typedef unsigned u32x4 __attribute__((ext_vector_type(4)));
typedef float f32x4 __attribute__((ext_vector_type(4)));
typedef __bf16 bf16x2 __attribute__((ext_vector_type(2)));
typedef unsigned long long u64;
#define GAS __attribute__((address_space(1)))
typedef GAS unsigned gu32;
typedef GAS u64 gu64;
#define RLX_AGENT __ATOMIC_RELAXED, __HIP_MEMORY_SCOPE_AGENT

constexpr int B = 4, D = 512, H = 32, SK = 8192, DH = 128, F = 1792;
constexpr int NWG = 256, NW = 8, NT = NW * 64;
constexpr int KEYS_WG = SK / 2;
constexpr int NIT = KEYS_WG / 16 / NW;
#define AUX_KV 2
#define AUX_W 2
#define POLL_T 8
#define STAMPS 0
constexpr size_t WS_EPOCH = 0;
constexpr size_t WS_ML = 4096;
constexpr size_t WS_H = WS_ML + (size_t)NWG * NW * 16;
constexpr size_t WS_Y = WS_H + (size_t)B * (F / 2) * 8;
constexpr size_t WS_END = WS_Y + (size_t)NWG * D * 8;
constexpr size_t WS_STAMP = 4u << 20;
constexpr unsigned SPIN_CAP = 1u << 20;

__device__ __forceinline__ float bf2f(unsigned v) { return __uint_as_float(v << 16); }
__device__ __forceinline__ float rbf(float x) { __bf16 b = (__bf16)x; return (float)b; }
__device__ __forceinline__ unsigned f2bf(float x) { __bf16 b = (__bf16)x; return __builtin_bit_cast(unsigned short, b); }
__device__ __forceinline__ float lo16(unsigned v) { return __uint_as_float(v << 16); }
__device__ __forceinline__ float hi16(unsigned v) { return __uint_as_float(v & 0xffff0000u); }
__device__ __forceinline__ float dot2(unsigned k, unsigned q, float acc) {
  return __builtin_amdgcn_fdot2_f32_bf16(__builtin_bit_cast(bf16x2, k), __builtin_bit_cast(bf16x2, q), acc, false);
}
template <int CTRL> __device__ __forceinline__ float dpp(float x) {
  return __builtin_bit_cast(float, __builtin_amdgcn_mov_dpp(__builtin_bit_cast(int, x), CTRL, 0xf, 0xf, true));
}
constexpr int XOR1 = 0xB1, XOR2 = 0x4E, XOR7 = 0x141, XOR8 = 0x128;
__device__ __forceinline__ float row_sum16(float x) { x += dpp<XOR1>(x); x += dpp<XOR2>(x); x += dpp<XOR7>(x); x += dpp<XOR8>(x); return x; }
__device__ __forceinline__ float tr32(float a, float b) {
  auto s = __builtin_amdgcn_permlane32_swap(__float_as_uint(a), __float_as_uint(b), false, false);
  return __uint_as_float(s[0]) + __uint_as_float(s[1]);
}
__device__ __forceinline__ float tr16(float a, float b) {
  auto s = __builtin_amdgcn_permlane16_swap(__float_as_uint(a), __float_as_uint(b), false, false);
  return __uint_as_float(s[0]) + __uint_as_float(s[1]);
}
template <int CTRL> __device__ __forceinline__ float trd(float a, float b, bool hi) {
  const float keep = hi ? b : a, send = hi ? a : b;
  return keep + dpp<CTRL>(send);
}
__device__ __forceinline__ float xrow_sum(float x) { x = tr16(x, x); return tr32(x, x); }
__device__ __forceinline__ float xrow_max(float x) {
  auto s = __builtin_amdgcn_permlane16_swap(__float_as_uint(x), __float_as_uint(x), false, false);
  x = fmaxf(__uint_as_float(s[0]), __uint_as_float(s[1]));
  auto t = __builtin_amdgcn_permlane32_swap(__float_as_uint(x), __float_as_uint(x), false, false);
  return fmaxf(__uint_as_float(t[0]), __uint_as_float(t[1]));
}
__device__ __forceinline__ float wave_sum(float x) { return xrow_sum(row_sum16(x)); }
__device__ __forceinline__ float reduce8_all(const float (&v)[8], int lane) {
  const float a0 = tr32(v[0], v[4]), a1 = tr32(v[1], v[5]), a2 = tr32(v[2], v[6]), a3 = tr32(v[3], v[7]);
  const float b0 = tr16(a0, a2), b1 = tr16(a1, a3);
  float c = trd<XOR8>(b0, b1, lane & 8);
  c += dpp<XOR7>(c); c += dpp<XOR2>(c); c += dpp<XOR1>(c);
  return c;
}
__device__ __forceinline__ float silu_f(float x) { return x / (1.f + __expf(-x)); }
__device__ __forceinline__ u64 granule(unsigned value, unsigned tag) { return ((u64)tag << 32) | value; }
#define RSRC(p, n) __builtin_amdgcn_make_buffer_rsrc((void*)(p), 0, (int)(n), 0x00020000)
#define BLOAD(r, vo, so, aux) __builtin_amdgcn_raw_buffer_load_b128((r), (vo), (so), (aux))
#define STAMP(i) do {} while (0)

constexpr int L_XS = 0;
constexpr int L_QP = L_XS + NW * 64;
constexpr int L_FP = L_QP + NW * DH;
constexpr int L_REC = L_FP + NW * 64;
constexpr int L_ML = L_REC + NW * DH;
constexpr int L_OT = L_ML + 16;
constexpr int L_F2 = L_OT + NW * DH;
constexpr int L_CNT = L_F2 + NW * 8;
constexpr int L_END = L_CNT + 4;

__global__ void __launch_bounds__(NT, 2) fused_decode(const unsigned short* __restrict__ x, const unsigned short* __restrict__ kc, const unsigned short* __restrict__ vc,
                                                      const unsigned short* __restrict__ wq, const unsigned short* __restrict__ wo, const unsigned short* __restrict__ wg,
                                                      const unsigned short* __restrict__ wu, const unsigned short* __restrict__ wd, unsigned short* __restrict__ out,
                                                      unsigned char* __restrict__ ws) {
  __shared__ __attribute__((aligned(16))) float lds[L_END];
  const int tid = threadIdx.x, lane = tid & 63, wave = __builtin_amdgcn_readfirstlane(tid >> 6);
  const int r = lane >> 4, c = lane & 15;
  const int x8 = blockIdx.x & 7, i32 = blockIdx.x >> 3;
  const int h = 4 * x8 + (i32 >> 3), b = (i32 >> 1) & 3, sp = i32 & 1;
  const int slot = b * 64 + 2 * h + sp;
  const int cb = 8 * x8 + (i32 & 7), bc = i32 >> 3;
  const int j1 = 28 * x8 + i32;
  STAMP(0);

  const unsigned epoch = __hip_atomic_load((gu32*)(ws + WS_EPOCH), RLX_AGENT);
  const auto rx = RSRC(x, B * D * 2);
  u32x4 xv[4]; unsigned xk[4];
#pragma unroll
  for (int t = 0; t < 4; ++t) xv[t] = BLOAD(rx, lane * 16, t * 1024, 0);
  __builtin_amdgcn_sched_barrier(0);
#pragma unroll
  for (int t = 0; t < 4; ++t) xk[t] = __builtin_amdgcn_raw_buffer_load_b16(rx, tid * 2, t * 1024, 0);
  __builtin_amdgcn_sched_barrier(0);
  const auto rwq = RSRC(wq + h * DH, D * H * DH * 2 - h * DH * 2);
  u32x4 Wq[16];
#pragma unroll
  for (int i = 0; i < 16; ++i) Wq[i] = BLOAD(rwq, (16 * r) * 8192 + c * 16, (64 * wave + i) * 8192, AUX_W);
  __builtin_amdgcn_sched_barrier(0);
  const size_t kvbase = ((size_t)(b * H + h) * SK + (size_t)sp * KEYS_WG) * DH;
  const auto rk = RSRC(kc + kvbase, KEYS_WG * DH * 2);
  const auto rv = RSRC(vc + kvbase, KEYS_WG * DH * 2);
  u32x4 K[4], V[4];
#define ISSUE_K(it) do { const unsigned so_ = ((it) * NW + wave) * 4096; _Pragma("unroll") for (int t = 0; t < 4; ++t) K[t] = BLOAD(rk, lane * 16 + t * 1024, so_, AUX_KV); } while (0)
#define ISSUE_V(it) do { const unsigned so_ = ((it) * NW + wave) * 4096; _Pragma("unroll") for (int t = 0; t < 4; ++t) V[t] = BLOAD(rv, lane * 16 + t * 1024, so_, AUX_KV); } while (0)
  ISSUE_K(0);
  __builtin_amdgcn_sched_barrier(0);
  ISSUE_V(0);
  __builtin_amdgcn_sched_barrier(0);
  const int ffn1_bytes = i32 < 28 ? D * F * 2 : 0;
  const u32x4 Wg = BLOAD(RSRC(wg, ffn1_bytes), tid * (F * 2), j1 * 16, AUX_W);
  const u32x4 Wu = BLOAD(RSRC(wu, ffn1_bytes), tid * (F * 2), j1 * 16, AUX_W);
  __builtin_amdgcn_sched_barrier(0);
  const auto rwo = RSRC(wo + (size_t)h * DH * D, DH * D * 2);
  u32x4 Wo[16];
#pragma unroll
  for (int i = 0; i < 16; ++i) Wo[i] = BLOAD(rwo, (lane >> 3) * 1024 + (lane & 7) * 16, i * 8192 + wave * 128, AUX_W);
  __builtin_amdgcn_sched_barrier(0);
  const auto rwd = RSRC(wd, F * D * 2);
  u32x4 Wd[4];
#pragma unroll
  for (int t = 0; t < 4; ++t) Wd[t] = BLOAD(rwd, (lane < 56 ? lane * 4096 : 0x40000000) + t * 1024, wave * 224 * 1024 + cb * 16, AUX_W);
  if (tid == 0) reinterpret_cast<unsigned*>(lds)[L_CNT] = 0u;
  asm volatile("" ::: "memory");
  __builtin_amdgcn_sched_barrier(0);

  float xn[4];
#pragma unroll
  for (int t = 0; t < 4; ++t) {
    float e[8];
#pragma unroll
    for (int k = 0; k < 4; ++k) { e[2 * k] = lo16(xv[t][k]); e[2 * k + 1] = hi16(xv[t][k]); }
    const float mean = wave_sum(((e[0] + e[1]) + (e[2] + e[3])) + ((e[4] + e[5]) + (e[6] + e[7]))) * (1.f / D);
    float q2 = 0.f;
#pragma unroll
    for (int k = 0; k < 8; ++k) { const float d = e[k] - mean; q2 = fmaf(d, d, q2); }
    const float var = rbf(wave_sum(q2) * (1.f / D));
    const float den = rbf(sqrtf(rbf(var + 1e-5f)));
    xn[t] = rbf(rbf(bf2f(xk[t]) - rbf(mean)) / den);
  }
  lds[L_XS + wave * 64 + lane] = b == 0 ? xn[0] : b == 1 ? xn[1] : b == 2 ? xn[2] : xn[3];
  STAMP(1);
  {
    float xr[16];
#pragma unroll
    for (int u = 0; u < 4; ++u) { const f32x4 t4 = *reinterpret_cast<const f32x4*>(lds + L_XS + wave * 64 + 16 * r + 4 * u); xr[4 * u] = t4[0]; xr[4 * u + 1] = t4[1]; xr[4 * u + 2] = t4[2]; xr[4 * u + 3] = t4[3]; }
    float a[8];
#pragma unroll
    for (int k = 0; k < 8; ++k) a[k] = 0.f;
#pragma unroll
    for (int i = 0; i < 16; ++i)
#pragma unroll
      for (int k = 0; k < 4; ++k) { a[2 * k] = fmaf(xr[i], lo16(Wq[i][k]), a[2 * k]); a[2 * k + 1] = fmaf(xr[i], hi16(Wq[i][k]), a[2 * k + 1]); }
    const float s0 = tr32(a[0], a[4]), s1 = tr32(a[1], a[5]), s2 = tr32(a[2], a[6]), s3 = tr32(a[3], a[7]);
    const float t0 = tr16(s0, s2), t1 = tr16(s1, s3);
    *reinterpret_cast<float2*>(lds + L_QP + wave * DH + 8 * c + 2 * r) = make_float2(t0, t1);
  }
  __syncthreads();
  u32x4 qv;
  {
    f32x4 s0 = {0.f, 0.f, 0.f, 0.f}, s1 = {0.f, 0.f, 0.f, 0.f};
#pragma unroll
    for (int w = 0; w < NW; ++w) { s0 += *reinterpret_cast<const f32x4*>(lds + L_QP + w * DH + 8 * c); s1 += *reinterpret_cast<const f32x4*>(lds + L_QP + w * DH + 8 * c + 4); }
    qv[0] = f2bf(s0[0]) | (f2bf(s0[1]) << 16); qv[1] = f2bf(s0[2]) | (f2bf(s0[3]) << 16);
    qv[2] = f2bf(s1[0]) | (f2bf(s1[1]) << 16); qv[3] = f2bf(s1[2]) | (f2bf(s1[3]) << 16);
  }
  unsigned tag = __builtin_amdgcn_readfirstlane(epoch) + 1u;
  STAMP(2);

  constexpr float SCALE = 0.088388347648318440f, LOG2E = 1.4426950408889634f;
  float m = -INFINITY, l = 0.f, acc[8];
#pragma unroll
  for (int k = 0; k < 8; ++k) acc[k] = 0.f;
#define BODY(it) do { \
    float s_[4]; \
    _Pragma("unroll") for (int t = 0; t < 4; ++t) { float d_ = dot2(K[t][0], qv[0], 0.f); d_ = dot2(K[t][1], qv[1], d_); d_ = dot2(K[t][2], qv[2], d_); s_[t] = dot2(K[t][3], qv[3], d_); } \
    asm volatile("" : "+v"(s_[0]), "+v"(s_[1]), "+v"(s_[2]), "+v"(s_[3])); \
    __builtin_amdgcn_sched_barrier(0); \
    ISSUE_K((it) + 1); \
    __builtin_amdgcn_sched_barrier(0); \
    _Pragma("unroll") for (int t = 0; t < 4; ++t) s_[t] = rbf(rbf(row_sum16(s_[t])) * SCALE) * LOG2E; \
    const float mn_ = fmaxf(fmaxf(fmaxf(s_[0], s_[1]), fmaxf(s_[2], s_[3])), m); \
    const float al_ = __builtin_amdgcn_exp2f(m - mn_); \
    m = mn_; \
    _Pragma("unroll") for (int t = 0; t < 4; ++t) s_[t] = __builtin_amdgcn_exp2f(s_[t] - mn_); \
    l = fmaf(l, al_, (s_[0] + s_[1]) + (s_[2] + s_[3])); \
    _Pragma("unroll") for (int k = 0; k < 8; ++k) acc[k] *= al_; \
    _Pragma("unroll") for (int t = 0; t < 4; ++t) \
      _Pragma("unroll") for (int k = 0; k < 4; ++k) { acc[2 * k] = fmaf(s_[t], lo16(V[t][k]), acc[2 * k]); acc[2 * k + 1] = fmaf(s_[t], hi16(V[t][k]), acc[2 * k + 1]); } \
    asm volatile("" : "+v"(acc[0]), "+v"(acc[1]), "+v"(acc[2]), "+v"(acc[3]), "+v"(acc[4]), "+v"(acc[5]), "+v"(acc[6]), "+v"(acc[7])); \
    __builtin_amdgcn_sched_barrier(0); \
    ISSUE_V((it) + 1); \
    __builtin_amdgcn_sched_barrier(0); \
  } while (0)
  BODY(0);

  {
    float wv[16];
#pragma unroll
    for (int k = 0; k < 4; ++k) { wv[2 * k] = lo16(Wg[k]); wv[2 * k + 1] = hi16(Wg[k]); wv[8 + 2 * k] = lo16(Wu[k]); wv[8 + 2 * k + 1] = hi16(Wu[k]); }
    float p16[16];
#pragma unroll
    for (int k = 0; k < 16; ++k) p16[k] = tr16(tr32(xn[0] * wv[k], xn[2] * wv[k]), tr32(xn[1] * wv[k], xn[3] * wv[k]));
    float p8[8], p4[4], p2[2];
#pragma unroll
    for (int k = 0; k < 8; ++k) p8[k] = trd<XOR8>(p16[k], p16[k + 8], lane & 8);
#pragma unroll
    for (int k = 0; k < 4; ++k) p4[k] = trd<XOR7>(p8[k], p8[k + 4], lane & 4);
#pragma unroll
    for (int k = 0; k < 2; ++k) p2[k] = trd<XOR2>(p4[k], p4[k + 2], lane & 2);
    lds[L_FP + wave * 64 + lane] = trd<XOR1>(p2[0], p2[1], lane & 1);
    unsigned arr = 0;
    __builtin_amdgcn_fence(__ATOMIC_RELEASE, "workgroup");
    if (lane == 0) arr = __hip_atomic_fetch_add(reinterpret_cast<unsigned*>(lds) + L_CNT, 1u, __ATOMIC_RELAXED, __HIP_MEMORY_SCOPE_WORKGROUP);
    arr = __builtin_amdgcn_readfirstlane(arr);
    if (arr == NW - 1 && i32 < 28) {
      __builtin_amdgcn_fence(__ATOMIC_ACQUIRE, "workgroup");
      float s = 0.f;
#pragma unroll
      for (int w = 0; w < NW; ++w) s += lds[L_FP + w * 64 + lane];
      s = rbf(s);
      const float u = dpp<XOR8>(s);
      const float hv = rbf(rbf(silu_f(s)) * u);
      const float hn = dpp<XOR1>(hv);
      if ((lane & 9) == 0)
        __hip_atomic_store((gu64*)(ws + WS_H) + (lane >> 4) * (F / 2) + j1 * 4 + ((lane & 7) >> 1), granule(f2bf(hv) | (f2bf(hn) << 16), tag), RLX_AGENT);
    }
  }
  STAMP(3);
  const int itA = 6 + 2 * wave;
  int it = 1;
  for (; it < itA; ++it) BODY(it);

  {
    gu64* hg = (gu64*)(ws + WS_H) + bc * (F / 2) + wave * 112 + (lane < 56 ? lane * 2 : 0);
    u64 g0, g1;
    for (unsigned spins = 0;; ++spins) {
      g0 = __hip_atomic_load(hg, RLX_AGENT); g1 = __hip_atomic_load(hg + 1, RLX_AGENT);
      const bool ok = (unsigned)(g0 >> 32) == tag && (unsigned)(g1 >> 32) == tag;
      if (__all(ok) || spins > SPIN_CAP) break;
      __builtin_amdgcn_s_sleep(8);
    }
    const float hv[4] = {lo16((unsigned)g0), hi16((unsigned)g0), lo16((unsigned)g1), hi16((unsigned)g1)};
    float a[8];
#pragma unroll
    for (int k = 0; k < 8; ++k) a[k] = 0.f;
#pragma unroll
    for (int t = 0; t < 4; ++t)
#pragma unroll
      for (int k = 0; k < 4; ++k) { a[2 * k] = fmaf(hv[t], lo16(Wd[t][k]), a[2 * k]); a[2 * k + 1] = fmaf(hv[t], hi16(Wd[t][k]), a[2 * k + 1]); }
    const float f = reduce8_all(a, lane);
    if ((lane & 7) == 0) lds[L_F2 + wave * 8 + (lane >> 3)] = f;
  }
  const unsigned xo = __builtin_amdgcn_raw_buffer_load_b16(rx, (lane >> 3) * 2, bc * 1024 + cb * 16, 0);
  STAMP(4);
  for (; it < NIT; ++it) BODY(it);
  STAMP(5);

  {
    const float mw = xrow_max(m), sc = __builtin_amdgcn_exp2f(m - mw);
    const float lw = xrow_sum(l * sc);
    const float s0 = tr32(acc[0] * sc, acc[4] * sc), s1 = tr32(acc[1] * sc, acc[5] * sc), s2 = tr32(acc[2] * sc, acc[6] * sc), s3 = tr32(acc[3] * sc, acc[7] * sc);
    *reinterpret_cast<float2*>(lds + L_REC + wave * DH + 8 * c + 2 * r) = make_float2(tr16(s0, s2), tr16(s1, s3));
    if (lane == 0) { lds[L_ML + wave] = mw; lds[L_ML + 8 + wave] = lw; }
  }
  __syncthreads();
  float M, L;
  {
    const f32x4 m0 = *reinterpret_cast<const f32x4*>(lds + L_ML), m1 = *reinterpret_cast<const f32x4*>(lds + L_ML + 4);
    const f32x4 l0 = *reinterpret_cast<const f32x4*>(lds + L_ML + 8), l1 = *reinterpret_cast<const f32x4*>(lds + L_ML + 12);
    M = fmaxf(fmaxf(fmaxf(m0[0], m0[1]), fmaxf(m0[2], m0[3])), fmaxf(fmaxf(m1[0], m1[1]), fmaxf(m1[2], m1[3])));
    float o0 = 0.f, o1 = 0.f; L = 0.f;
#pragma unroll
    for (int w = 0; w < NW; ++w) {
      const float sc = __builtin_amdgcn_exp2f((w < 4 ? m0[w & 3] : m1[w & 3]) - M);
      const float2 v = *reinterpret_cast<const float2*>(lds + L_REC + w * DH + 2 * lane);
      L = fmaf(sc, w < 4 ? l0[w & 3] : l1[w & 3], L); o0 = fmaf(sc, v.x, o0); o1 = fmaf(sc, v.y, o1);
    }
    float* ot = lds + L_OT + wave * DH;
    ot[((2 * lane) & 7) * 16 + (lane >> 2)] = o0;
    ot[(((2 * lane) & 7) + 1) * 16 + (lane >> 2)] = o1;
    float orow[16];
#pragma unroll
    for (int u = 0; u < 4; ++u) { const f32x4 t4 = *reinterpret_cast<const f32x4*>(ot + (lane >> 3) * 16 + 4 * u); orow[4 * u] = t4[0]; orow[4 * u + 1] = t4[1]; orow[4 * u + 2] = t4[2]; orow[4 * u + 3] = t4[3]; }
    float a[8];
#pragma unroll
    for (int k = 0; k < 8; ++k) a[k] = 0.f;
#pragma unroll
    for (int i = 0; i < 16; ++i)
#pragma unroll
      for (int k = 0; k < 4; ++k) { a[2 * k] = fmaf(orow[i], lo16(Wo[i][k]), a[2 * k]); a[2 * k + 1] = fmaf(orow[i], hi16(Wo[i][k]), a[2 * k + 1]); }
    const float a0 = tr32(a[0], a[4]), a1 = tr32(a[1], a[5]), a2 = tr32(a[2], a[6]), a3 = tr32(a[3], a[7]);
    const float y = trd<XOR8>(tr16(a0, a2), tr16(a1, a3), lane & 8);
    __hip_atomic_store((gu64*)(ws + WS_Y) + slot * D + 64 * wave + 8 * (lane & 7) + (lane >> 3), granule(__float_as_uint(y), tag), RLX_AGENT);
    if (lane < 2) __hip_atomic_store((gu64*)(ws + WS_ML) + (slot * NW + wave) * 2 + lane, granule(__float_as_uint(lane ? L : M), tag), RLX_AGENT);
  }
  STAMP(6);

  if (wave == 0) {
    float f = 0.f;
#pragma unroll
    for (int w = 0; w < NW; ++w) f += lds[L_F2 + w * 8 + (lane >> 3)];
    const float gate = rbf(silu_f(rbf(f)));
    gu64* mlp = (gu64*)(ws + WS_ML) + ((bc * 64 + lane) * NW + (cb >> 3)) * 2;
    gu64* yp = (gu64*)(ws + WS_Y) + (bc * 64 + lane) * D + 8 * cb;
    unsigned spins = 0;
    bool ok = false;
    for (;; ++spins) {
      if (!ok) ok = (unsigned)(__hip_atomic_load(mlp + 1, RLX_AGENT) >> 32) == tag;
      if (__popcll(__ballot(!ok)) <= POLL_T || spins > SPIN_CAP) break;
      __builtin_amdgcn_s_sleep(8);
    }
    u64 gm = 0, gl = 0, gy[8] = {0, 0, 0, 0, 0, 0, 0, 0};
    ok = false;
    for (;; ++spins) {
      if (!ok) {
        gm = __hip_atomic_load(mlp, RLX_AGENT); gl = __hip_atomic_load(mlp + 1, RLX_AGENT);
#pragma unroll
        for (int k = 0; k < 8; ++k) gy[k] = __hip_atomic_load(yp + k, RLX_AGENT);
        ok = (unsigned)(gm >> 32) == tag && (unsigned)(gl >> 32) == tag;
#pragma unroll
        for (int k = 0; k < 8; ++k) ok &= (unsigned)(gy[k] >> 32) == tag;
      }
      if (__all(ok) || spins > SPIN_CAP) break;
    }
    const float mp = __uint_as_float((unsigned)gm), lp = __uint_as_float((unsigned)gl);
    const float e = __builtin_amdgcn_exp2f(mp - fmaxf(mp, dpp<XOR1>(mp)));
    const float el = e * lp;
    const float wgt = e / (el + dpp<XOR1>(el));
    float a[8];
#pragma unroll
    for (int k = 0; k < 8; ++k) a[k] = wgt * __uint_as_float((unsigned)gy[k]);
    const float s = rbf(reduce8_all(a, lane));
    if ((lane & 7) == 0) out[bc * D + 8 * cb + (lane >> 3)] = (unsigned short)f2bf(bf2f(xo) + rbf(s * gate));
    STAMP(7);
  } else if (wave == 1 && blockIdx.x == 0) {
    gu64* mlp = (gu64*)(ws + WS_ML) + (lane * 4 * NW) * 2;
    for (unsigned spins = 0;; ++spins) {
      bool ok = true;
#pragma unroll
      for (int k = 0; k < 4; ++k) ok &= (unsigned)(__hip_atomic_load(mlp + k * NW * 2, RLX_AGENT) >> 32) == tag;
      if (__all(ok) || spins > SPIN_CAP) break;
      __builtin_amdgcn_s_sleep(2);
    }
    if (lane == 0) __hip_atomic_store((gu32*)(ws + WS_EPOCH), tag, RLX_AGENT);
  }
}

extern "C" void kernel_launch(void* const* d_in, const int* in_sizes, int n_in, void* d_out, int out_size, void* d_ws, size_t ws_size, hipStream_t stream) {
  for (int i = 0; i < n_in; ++i) { hipPointerAttribute_t at; hipError_t e = hipPointerGetAttributes(&at, d_in[i]); void* base = nullptr; size_t sz = 0; hipMemGetAddressRange((hipDeviceptr_t*)&base, &sz, d_in[i]);
    fprintf(stderr, "PTR in[%d] %p n=%d err=%d type=%d managed=%d flags=%u base=%p size=%zu\n", i, d_in[i], in_sizes[i], (int)e, (int)at.type, at.isManaged, at.allocationFlags, base, sz); }
  { void* base = nullptr; size_t sz = 0; hipMemGetAddressRange((hipDeviceptr_t*)&base, &sz, d_ws); fprintf(stderr, "PTR out %p ws %p wsbase %p size %zu\n", d_out, d_ws, base, sz); }
  fused_decode<<<NWG, NT, 0, stream>>>((const unsigned short*)d_in[0], (const unsigned short*)d_in[1], (const unsigned short*)d_in[2], (const unsigned short*)d_in[3],
                                       (const unsigned short*)d_in[4], (const unsigned short*)d_in[6], (const unsigned short*)d_in[7], (const unsigned short*)d_in[8],
                                       (unsigned short*)d_out, (unsigned char*)d_ws);
}
```

```cpp
#include <hip/hip_runtime.h>
#include <hip/hip_bf16.h>
#include <cstdint>

typedef unsigned u32x4 __attribute__((ext_vector_type(4)));
typedef unsigned u32x2 __attribute__((ext_vector_type(2)));
typedef float f32x4 __attribute__((ext_vector_type(4)));
typedef __bf16 bf16x2 __attribute__((ext_vector_type(2)));
typedef unsigned long long u64;
typedef __attribute__((address_space(1))) unsigned gu32;
#define RLX_AGENT __ATOMIC_RELAXED, __HIP_MEMORY_SCOPE_AGENT
#define RLX_WG __ATOMIC_RELAXED, __HIP_MEMORY_SCOPE_WORKGROUP

constexpr int B = 4, D = 512, H = 32, SK = 8192, DH = 128, F = 1792;
constexpr int NWG = 256, NW = 8, NT = NW * 64;
constexpr int NS = 4;
constexpr int NLD = SK / 2 / 4 / NS;
#define PACE 7100
#define PACE0 350
#define LEAK 2
#define HX_ 20
#define HY_ 12
#define NE 7
#define T_Q 540
constexpr int NG = 7, NMID = 6, NLO = 5;
constexpr int NR = NG + 1;
#define MIDW_ 200
constexpr int MIDW = MIDW_;
constexpr int HX = HX_, HY = HY_;
constexpr int HK = HX + HY, NKC = NLD - HK;
constexpr int T_H = 900, T_DN = 1400, T_WO = 1600, T_GATE = 3800, T_ML = 4600;
constexpr int NT_AUX = 2, SC1_AUX = 16;
constexpr unsigned WS_EPOCH = 0;
constexpr unsigned WS_H = 4096;
constexpr unsigned WS_ML = WS_H + B * (F / 2) * 8;
constexpr unsigned WS_Y = WS_ML + NWG * 128;
constexpr unsigned WS_Q = WS_Y + NWG * 64 * 64;
constexpr unsigned WS_GU = WS_Q + 128 * 512 * 8;
constexpr unsigned WS_DN = WS_GU + 28 * 4 * 512 * 8;
constexpr unsigned WS_END = WS_DN + NWG * 256 * 8;
constexpr unsigned SPIN_CAP = 1u << 20;
constexpr int L_P = 0;
constexpr int L_XS = L_P + NS * 1024;
constexpr int L_HQ = L_XS + 4 * 64;
constexpr int L_QP = L_HQ + 4 * 512;
constexpr int L_HS = L_QP + 4 * DH;
constexpr int L_REC = L_HS + 256;
constexpr int L_ML = L_REC + NS * DH;
constexpr int L_OT = L_ML + 8;
constexpr int L_FIN = L_OT + NW * DH;
constexpr int L_CNT = L_FIN + 48;
constexpr int L_WO = L_CNT + 16;
constexpr int L_END = L_WO + 4 * 16 * 256;
enum { C_HQ = 0, C_FIN = 1, C_ML = 2, C_TAG = 3, C_SC = 4, C_WO = 8, C_NRM = 12, C_MW = 13 };

__device__ __forceinline__ float rbf(float x) { __bf16 b = (__bf16)x; return (float)b; }
__device__ __forceinline__ unsigned f2bf(float x) { __bf16 b = (__bf16)x; return __builtin_bit_cast(unsigned short, b); }
__device__ __forceinline__ unsigned pk2bf(float lo, float hi) { return __builtin_bit_cast(unsigned, bf16x2{(__bf16)lo, (__bf16)hi}); }
__device__ __forceinline__ float lo16(unsigned v) { return __uint_as_float(v << 16); }
__device__ __forceinline__ float hi16(unsigned v) { return __uint_as_float(v & 0xffff0000u); }
__device__ __forceinline__ float dot2(unsigned k, unsigned q, float acc) {
  return __builtin_amdgcn_fdot2_f32_bf16(__builtin_bit_cast(bf16x2, k), __builtin_bit_cast(bf16x2, q), acc, false);
}
__device__ __forceinline__ void pair_rows(u32x4& a, u32x4& b) {
#pragma unroll
  for (int k = 0; k < 4; ++k) {
    const unsigned lo = __builtin_amdgcn_perm(b[k], a[k], 0x05040100u), hi = __builtin_amdgcn_perm(b[k], a[k], 0x07060302u);
    a[k] = lo; b[k] = hi;
  }
}
template <int CTRL> __device__ __forceinline__ float dpp(float x) {
  return __builtin_bit_cast(float, __builtin_amdgcn_mov_dpp(__builtin_bit_cast(int, x), CTRL, 0xf, 0xf, true));
}
constexpr int XOR1 = 0xB1, XOR2 = 0x4E, XOR7 = 0x141, XOR8 = 0x128, SHL4 = 0x104;
__device__ __forceinline__ float row_sum16(float x) { x += dpp<XOR8>(x); x += dpp<XOR7>(x); x += dpp<XOR2>(x); x += dpp<XOR1>(x); return x; }
__device__ __forceinline__ float tr32(float a, float b) {
  auto s = __builtin_amdgcn_permlane32_swap(__float_as_uint(a), __float_as_uint(b), false, false);
  return __uint_as_float(s[0]) + __uint_as_float(s[1]);
}
__device__ __forceinline__ float tr16(float a, float b) {
  auto s = __builtin_amdgcn_permlane16_swap(__float_as_uint(a), __float_as_uint(b), false, false);
  return __uint_as_float(s[0]) + __uint_as_float(s[1]);
}
__device__ __forceinline__ float tr_rows(float a, float b, float c, float d) { return tr16(tr32(a, c), tr32(b, d)); }
template <int CTRL> __device__ __forceinline__ float trd(float a, float b, bool hi) {
  const float keep = hi ? b : a, send = hi ? a : b;
  return keep + dpp<CTRL>(send);
}
__device__ __forceinline__ float wave_sum(float x) { x = row_sum16(x); x = tr16(x, x); return tr32(x, x); }
__device__ __forceinline__ float wave_max(float x) {
  x = fmaxf(x, dpp<XOR1>(x)); x = fmaxf(x, dpp<XOR2>(x)); x = fmaxf(x, dpp<XOR7>(x)); x = fmaxf(x, dpp<XOR8>(x));
  auto s = __builtin_amdgcn_permlane16_swap(__float_as_uint(x), __float_as_uint(x), false, false);
  x = fmaxf(__uint_as_float(s[0]), __uint_as_float(s[1]));
  auto t = __builtin_amdgcn_permlane32_swap(__float_as_uint(x), __float_as_uint(x), false, false);
  return fmaxf(__uint_as_float(t[0]), __uint_as_float(t[1]));
}
__device__ __forceinline__ float silu_f(float x) { return x / (1.f + __expf(-x)); }
#define RSRC(p, n) __builtin_amdgcn_make_buffer_rsrc((void*)(p), 0, (int)(n), 0x00020000)
#define BLOAD(r, vo, so, aux) __builtin_amdgcn_raw_buffer_load_b128((r), (vo), (so), (aux))
#define SB() __builtin_amdgcn_sched_barrier(0)
#define BAR() do { asm volatile("s_waitcnt lgkmcnt(0)" ::: "memory"); __builtin_amdgcn_s_barrier(); asm volatile("" ::: "memory"); SB(); } while (0)

__device__ __forceinline__ void lds_arrive(float* lds, int idx, int lane) {
  __builtin_amdgcn_fence(__ATOMIC_RELEASE, "workgroup");
  if (lane == 0) __hip_atomic_fetch_add(reinterpret_cast<unsigned*>(lds) + L_CNT + idx, 1u, RLX_WG);
}
template <int SLEEP> __device__ __forceinline__ void lds_wait(float* lds, int idx, unsigned n) {
  while (__hip_atomic_load(reinterpret_cast<unsigned*>(lds) + L_CNT + idx, RLX_WG) < n) __builtin_amdgcn_s_sleep(SLEEP);
  __builtin_amdgcn_fence(__ATOMIC_ACQUIRE, "workgroup");
}
template <int SLEEP> __device__ __forceinline__ void wait_until(u64 t) { while (__builtin_amdgcn_s_memrealtime() < t) __builtin_amdgcn_s_sleep(SLEEP); }
__device__ __forceinline__ float2 sum4(const float* p, int stride) {
  float2 s = *reinterpret_cast<const float2*>(p);
#pragma unroll
  for (int w = 1; w < 4; ++w) { const float2 t = *reinterpret_cast<const float2*>(p + w * stride); s.x += t.x; s.y += t.y; }
  return s;
}
#define TAGOK(p) ((p)[1] == tag && (p)[3] == tag)
#define GV0(p) __uint_as_float((p)[0])
#define GV1(p) __uint_as_float((p)[2])
#define PUB2(off, a, b) __builtin_amdgcn_raw_buffer_store_b128(u32x4{(a), tag, (b), tag}, rws, (off), 0, SC1_AUX)
#define PUB2F(off, a, b) PUB2(off, __float_as_uint(a), __float_as_uint(b))
#define GET2(off) BLOAD(rws, (off), 0, SC1_AUX)
#define FRESH(o) asm volatile("" : "+v"(o) :: "memory")
#define UNTIL_TAGGED(OK, o, LOADS, SLEEP) do { if (!__all(OK)) { \
    _Pragma("nounroll") for (unsigned spins_ = 0; spins_ < SPIN_CAP; ++spins_) { \
      if (SLEEP) __builtin_amdgcn_s_sleep(SLEEP); FRESH(o); LOADS; if (__all(OK)) break; } } } while (0)

constexpr float SCALE = 0.088388347648318440f, LOG2E = 1.4426950408889634f;
__device__ __forceinline__ float score(const u32x4& k, const u32x4& q) {
  return rbf(rbf(row_sum16(dot2(k[3], q[3], dot2(k[2], q[2], dot2(k[1], q[1], dot2(k[0], q[0], 0.f)))))) * SCALE);
}
__device__ __forceinline__ u32x4 gather_q(const float* lds, int c) {
  f32x4 s0 = *reinterpret_cast<const f32x4*>(lds + L_QP + 8 * c), s1 = *reinterpret_cast<const f32x4*>(lds + L_QP + 8 * c + 4);
#pragma unroll
  for (int w = 1; w < 4; ++w) { s0 += *reinterpret_cast<const f32x4*>(lds + L_QP + w * DH + 8 * c); s1 += *reinterpret_cast<const f32x4*>(lds + L_QP + w * DH + 8 * c + 4); }
  return u32x4{pk2bf(s0[0], s0[1]), pk2bf(s0[2], s0[3]), pk2bf(s1[0], s1[1]), pk2bf(s1[2], s1[3])};
}

__global__ void __launch_bounds__(NT, 2) fused_decode(const unsigned short* __restrict__ x, const unsigned short* __restrict__ kc, const unsigned short* __restrict__ vc,
                                                      const unsigned short* __restrict__ wq, const unsigned short* __restrict__ wo, const unsigned short* __restrict__ wg,
                                                      const unsigned short* __restrict__ wu, const unsigned short* __restrict__ wd, unsigned short* __restrict__ out,
                                                      unsigned char* ws) {
  __shared__ __attribute__((aligned(16))) float lds[L_END];
  const u64 tstart = __builtin_amdgcn_s_memrealtime();
  asm volatile("" :: "s"(x), "s"(kc), "s"(vc), "s"(wq), "s"(wo), "s"(wg), "s"(wu), "s"(wd), "s"(out), "s"(ws));
  const int tid = threadIdx.x, lane = tid & 63, wave = __builtin_amdgcn_readfirstlane(tid >> 6), sw = wave & 3;
  const int r = lane >> 4, c = lane & 15, g8 = lane >> 3, c8 = lane & 7;
  const int i32 = blockIdx.x >> 3;
  const int h = (blockIdx.x & 7) + 8 * (i32 >> 3), b = (i32 >> 1) & 3, sp = i32 & 1;
  const int ks = 2 * b + sp, slot = 2 * h + sp;
  const int cb = blockIdx.x & 63, bc = blockIdx.x >> 6;
  const auto rws = RSRC(ws, WS_END);
  const size_t kvbase = (size_t)(b * H + h) * SK * DH;
  const auto rk = RSRC(kc + kvbase, SK * DH * 2);
  const int kvo = sp * 32768 + sw * 1024;
#define KV_LOAD(rs, i) BLOAD(rs, lane * 16, ((i) >> 3) * 65536 + ((i) & 7) * 4096 + kvo, NT_AUX)
  float* pw = lds + L_P + sw * 1024;
  u32x4 Wo[16], R[NR];
  unsigned tag;

  if (wave >= 4) {
#pragma unroll
    for (int g = 0; g < NE; ++g) R[g] = KV_LOAD(rk, HK + g);
  }
  SB();
  if (__builtin_expect(wave < 4, 1)) {
    __builtin_amdgcn_s_setprio(2);
    const auto rx = RSRC(x, B * D * 2);
    const int kq = ks >> 1, k0 = 128 * kq + 32 * wave;
    u32x4 xv[4], W[8], Kh[HY];
#pragma unroll
    for (int t = 0; t < 4; ++t) xv[t] = BLOAD(rx, r * 1024 + c * 16, t * 256, 0);
    const unsigned xk = __builtin_amdgcn_raw_buffer_load_b32(rx, r * 1024 + c * 4, k0 * 2, 0);
    const unsigned epoch = __hip_atomic_load((gu32*)(ws + WS_EPOCH), RLX_AGENT);
    SB();
    {
      const int gubytes = h < 28 ? D * F * 2 : 0;
      const auto r0 = sp ? RSRC(wg, gubytes) : RSRC(wq, D * H * DH * 2), r1 = sp ? RSRC(wu, gubytes) : r0;
      const int rowb = sp ? F * 2 : H * DH * 2;
      const int vo = sp ? 4 * g8 * (F * 2) + c8 * 16 : 8 * r * (H * DH * 2) + c * 16;
      const int so = k0 * rowb + (sp ? h * 128 : h * 256);
#pragma unroll
      for (int i = 0; i < 4; ++i) W[i] = BLOAD(r0, vo, so + i * rowb, NT_AUX);
#pragma unroll
      for (int i = 0; i < 4; ++i) W[4 + i] = BLOAD(r1, vo, so + (sp ? i : 4 + i) * rowb, NT_AUX);
    }
    if (tid < 16) reinterpret_cast<unsigned*>(lds)[L_CNT + tid] = 0u;
    BAR();
    {
      float s4[4] = {0.f, 0.f, 0.f, 0.f}, v4[4] = {0.f, 0.f, 0.f, 0.f};
#pragma unroll
      for (int t = 0; t < 4; ++t)
#pragma unroll
        for (int k = 0; k < 4; ++k) { s4[k] = dot2(xv[t][k], 0x3f803f80u, s4[k]); v4[k] = dot2(xv[t][k], xv[t][k], v4[k]); }
      const float mean = row_sum16((s4[0] + s4[1]) + (s4[2] + s4[3])) * (1.f / D);
      float var = fmaf(-mean, mean, row_sum16((v4[0] + v4[1]) + (v4[2] + v4[3])) * (1.f / D));
      if (__builtin_expect(__any(!(mean * mean <= 64.f * var)), 0)) {
#pragma unroll
        for (int k = 0; k < 4; ++k) v4[k] = 0.f;
#pragma unroll
        for (int t = 0; t < 4; ++t)
#pragma unroll
          for (int k = 0; k < 4; ++k) { const float d0 = lo16(xv[t][k]) - mean, d1 = hi16(xv[t][k]) - mean; v4[k] = fmaf(d1, d1, fmaf(d0, d0, v4[k])); }
        var = row_sum16((v4[0] + v4[1]) + (v4[2] + v4[3])) * (1.f / D);
      }
      const float den = rbf(sqrtf(rbf(rbf(var) + 1e-5f))), mr = rbf(mean);
      reinterpret_cast<unsigned*>(lds + L_XS + wave * 64)[lane] = pk2bf(rbf(lo16(xk) - mr) / den, rbf(hi16(xk) - mr) / den);
    }
    tag = __builtin_amdgcn_readfirstlane(epoch) + 1u;
    const unsigned* xs = reinterpret_cast<const unsigned*>(lds + L_XS + wave * 64);
#pragma unroll
    for (int i = 0; i < 8; i += 2) pair_rows(W[i], W[i + 1]);
    if (__builtin_expect(sp == 0, 1)) {
      float a[4][8];
#pragma unroll
      for (int bb = 0; bb < 4; ++bb) {
        const u32x4 xr = *reinterpret_cast<const u32x4*>(xs + bb * 16 + 4 * r);
#pragma unroll
        for (int k = 0; k < 8; ++k)
          a[bb][k] = dot2(W[6 + (k & 1)][k >> 1], xr[3], dot2(W[4 + (k & 1)][k >> 1], xr[2], dot2(W[2 + (k & 1)][k >> 1], xr[1], dot2(W[k & 1][k >> 1], xr[0], 0.f))));
      }
      f32x4 o0, o1;
#pragma unroll
      for (int j = 0; j < 4; ++j) { o0[j] = tr_rows(a[0][j], a[1][j], a[2][j], a[3][j]); o1[j] = tr_rows(a[0][4 + j], a[1][4 + j], a[2][4 + j], a[3][4 + j]); }
      *reinterpret_cast<f32x4*>(lds + L_HQ + wave * 512 + r * DH + 8 * c) = o0;
      *reinterpret_cast<f32x4*>(lds + L_HQ + wave * 512 + r * DH + 8 * c + 4) = o1;
    } else {
      float a[4][16];
#pragma unroll
      for (int bb = 0; bb < 4; ++bb) {
        const u32x2 xr = *reinterpret_cast<const u32x2*>(xs + bb * 16 + 2 * g8);
#pragma unroll
        for (int k = 0; k < 8; ++k) {
          a[bb][k] = dot2(W[2 + (k & 1)][k >> 1], xr[1], dot2(W[k & 1][k >> 1], xr[0], 0.f));
          a[bb][8 + k] = dot2(W[6 + (k & 1)][k >> 1], xr[1], dot2(W[4 + (k & 1)][k >> 1], xr[0], 0.f));
        }
      }
      float t16[16];
#pragma unroll
      for (int j = 0; j < 16; ++j) t16[j] = tr_rows(a[0][j], a[1][j], a[2][j], a[3][j]);
      f32x4 o0, o1;
#pragma unroll
      for (int j = 0; j < 4; ++j) { o0[j] = trd<XOR8>(t16[j], t16[8 + j], lane & 8); o1[j] = trd<XOR8>(t16[4 + j], t16[12 + j], lane & 8); }
      *reinterpret_cast<f32x4*>(lds + L_HQ + wave * 512 + c8 * 64 + g8 * 8) = o0;
      *reinterpret_cast<f32x4*>(lds + L_HQ + wave * 512 + c8 * 64 + g8 * 8 + 4) = o1;
    }
    lds_arrive(lds, C_HQ, lane);
    lds_wait<1>(lds, C_HQ, 4);
    {
      const float2 s = sum4(lds + L_HQ + wave * 128 + 2 * lane, 512);
      const unsigned po = sp ? WS_GU + (((h * 8 + 2 * wave + (lane >> 5)) * 4 + kq) * 64 + 2 * (lane & 31)) * 8 : WS_Q + (((h * 4 + wave) * 4 + kq) * DH + 2 * lane) * 8;
      if (sp == 0 || h < 28) PUB2F(po, s.x, s.y);
    }
    wait_until<1>(tstart + T_Q);
    unsigned qi = WS_Q + (((h * 4 + b) * 4 + wave) * DH + 2 * lane) * 8;
    FRESH(qi);
    u32x4 q0 = GET2(qi);
    SB();
#pragma unroll
    for (int i = 0; i < HY; ++i) Kh[i] = KV_LOAD(rk, HX + i);
    SB();
    UNTIL_TAGGED(TAGOK(q0), qi, q0 = GET2(qi), 0);
    *reinterpret_cast<float2*>(lds + L_QP + wave * DH + 2 * lane) = make_float2(GV0(q0), GV1(q0));
    if (tid == 0) reinterpret_cast<unsigned*>(lds)[L_CNT + C_TAG] = tag;
    BAR();
    __builtin_amdgcn_s_setprio(0);
    {
      const u32x4 qv = gather_q(lds, c);
#pragma unroll
      for (int i = 0; i < HY; ++i) pw[(HX + i) * 4 + r] = score(Kh[i], qv);
      lds_arrive(lds, C_SC + wave, lane);
    }
    if (wave == 0 && h < 28) {
      wait_until<4>(tstart + T_H);
      unsigned gi = WS_GU + (((h * 8 + ks) * 4) * 64 + 2 * lane) * 8;
      FRESH(gi);
      u32x4 g0 = GET2(gi), g1 = GET2(gi + 1024);
      UNTIL_TAGGED(TAGOK(g0) && TAGOK(g1), gi, (g0 = GET2(gi), g1 = GET2(gi + 1024)), 32);
      float s0 = GV0(g0) + GV0(g1), s1 = GV1(g0) + GV1(g1);
      s0 = rbf(tr32(s0, s0)); s1 = rbf(tr32(s1, s1));
      const unsigned hp = pk2bf(rbf(silu_f(s0)) * dpp<SHL4>(s0), rbf(silu_f(s1)) * dpp<SHL4>(s1));
      const unsigned hn = __builtin_amdgcn_mov_dpp(hp, XOR1, 0xf, 0xf, true);
      if (lane < 32 && (lane & 5) == 0) PUB2(WS_H + ((8 * h + ks) * 16 + g8 * 4 + (lane & 2)) * 8, hp, hn);
    }
    if (wave == 1) {
      const auto rwd = RSRC(wd, F * D * 2);
      u32x4 Wd[7];
      wait_until<8>(tstart + T_DN);
      unsigned hi = WS_H + (7 * h * 16 + 2 * (lane < 56 ? lane : 0)) * 8;
      FRESH(hi);
      u32x4 p = GET2(hi);
#pragma unroll
      for (int i = 0; i < 7; ++i) {
        Wd[i] = BLOAD(rwd, g8 * 7 * 1024 + c8 * 16, (56 * h + i) * 1024 + ks * 128, NT_AUX);
        asm volatile("s_waitcnt vmcnt(0)" ::: "memory"); SB();
      }
      UNTIL_TAGGED(TAGOK(p), hi, p = GET2(hi), 32);
      if (lane < 56) *reinterpret_cast<f32x4*>(lds + L_HS + ((lane >> 1) & 3) * 56 + g8 * 8 + (lane & 1) * 4) = f32x4{lo16(p[0]), hi16(p[0]), lo16(p[2]), hi16(p[2])};
      float a[4][8];
#pragma unroll
      for (int bb = 0; bb < 4; ++bb) {
#pragma unroll
        for (int k = 0; k < 8; ++k) a[bb][k] = 0.f;
#pragma unroll
        for (int i = 0; i < 7; ++i) {
          const float hv = lds[L_HS + bb * 56 + 7 * g8 + i];
#pragma unroll
          for (int k = 0; k < 4; ++k) { a[bb][2 * k] = fmaf(hv, lo16(Wd[i][k]), a[bb][2 * k]); a[bb][2 * k + 1] = fmaf(hv, hi16(Wd[i][k]), a[bb][2 * k + 1]); }
        }
      }
      f32x4 o4;
#pragma unroll
      for (int j = 0; j < 4; ++j)
        o4[j] = trd<XOR8>(tr_rows(a[0][j], a[1][j], a[2][j], a[3][j]), tr_rows(a[0][4 + j], a[1][4 + j], a[2][4 + j], a[3][4 + j]), lane & 8);
      *reinterpret_cast<f32x4*>(lds + L_HQ + r * 64 + 8 * c8 + ((lane & 8) >> 1)) = o4;
      const float2 d0 = *reinterpret_cast<const float2*>(lds + L_HQ + 2 * lane), d1 = *reinterpret_cast<const float2*>(lds + L_HQ + 128 + 2 * lane);
      PUB2F(WS_DN + ((ks * 32 + h) * 256 + 2 * lane) * 8, d0.x, d0.y);
      PUB2F(WS_DN + ((ks * 32 + h) * 256 + 128 + 2 * lane) * 8, d1.x, d1.y);
    }
    const auto rwo = RSRC(wo + (size_t)h * DH * D, DH * D * 2);
    wait_until<8>(tstart + T_WO);
#pragma nounroll
    for (int i = 0; i < 16; i += 2) {
      u32x4 t0 = BLOAD(rwo, g8 * 16384 + c8 * 16, i * 1024 + (wave + 4) * 128, NT_AUX);
      u32x4 t1 = BLOAD(rwo, g8 * 16384 + c8 * 16, i * 1024 + 1024 + (wave + 4) * 128, NT_AUX);
      pair_rows(t0, t1);
      *reinterpret_cast<u32x4*>(lds + L_WO + (wave * 16 + i) * 256 + lane * 4) = t0;
      *reinterpret_cast<u32x4*>(lds + L_WO + (wave * 16 + i + 1) * 256 + lane * 4) = t1;
    }
    lds_arrive(lds, C_WO + wave, lane);
#pragma unroll
    for (int i = 0; i < 16; i += 2) {
      Wo[i] = BLOAD(rwo, g8 * 16384 + c8 * 16, i * 1024 + wave * 128, NT_AUX);
      Wo[i + 1] = BLOAD(rwo, g8 * 16384 + c8 * 16, i * 1024 + 1024 + wave * 128, NT_AUX);
      pair_rows(Wo[i], Wo[i + 1]);
      asm volatile("" : "+v"(Wo[i]), "+v"(Wo[i + 1])); SB();
    }
    if (wave == 3) {
      lds_wait<8>(lds, C_ML, 4);
      const f32x4 m4 = *reinterpret_cast<const f32x4*>(lds + L_ML), l4 = *reinterpret_cast<const f32x4*>(lds + L_ML + 4);
      const float M = fmaxf(fmaxf(m4[0], m4[1]), fmaxf(m4[2], m4[3]));
      float L = 0.f;
#pragma unroll
      for (int w = 0; w < NS; ++w) L = fmaf(__builtin_amdgcn_exp2f(m4[w] - M), l4[w], L);
      if (lane == 0) PUB2F(WS_ML + (b * 64 + slot) * 128, M, L);
      wait_until<8>(tstart + T_ML);
      unsigned mi = WS_ML + (b * 64 + (slot ^ 1)) * 128;
      FRESH(mi);
      u32x4 o = GET2(mi);
      UNTIL_TAGGED(TAGOK(o), mi, o = GET2(mi), 32);
      const float mm = fmaxf(M, GV0(o));
      const float ea = __builtin_amdgcn_exp2f(M - mm) * L, eb = __builtin_amdgcn_exp2f(GV0(o) - mm) * GV1(o);
      if (lane == 0) { lds[L_CNT + C_MW] = mm; lds[L_CNT + C_MW + 1] = 1.f / (ea + eb); }
      lds_arrive(lds, C_NRM, lane);
    }
    if (wave == 0) {
      wait_until<8>(tstart + T_GATE);
      const float xres = lo16(__builtin_amdgcn_raw_buffer_load_b16(rx, g8 * 2, bc * 1024 + cb * 16, 0));
      unsigned di = WS_DN + ((((cb >> 3) * 32 + (lane >> 1)) * 4 + bc) * 64 + 8 * (cb & 7) + 4 * (lane & 1)) * 8;
      FRESH(di);
      u32x4 d0 = GET2(di), d1 = GET2(di + 16);
      UNTIL_TAGGED(TAGOK(d0) && TAGOK(d1), di, (d0 = GET2(di), d1 = GET2(di + 16)), 32);
      const bool up = lane & 1;
      const float e0 = tr32(up ? 0.f : GV0(d0), up ? GV0(d0) : 0.f), e1 = tr32(up ? 0.f : GV1(d0), up ? GV1(d0) : 0.f);
      const float e2 = tr32(up ? 0.f : GV0(d1), up ? GV0(d1) : 0.f), e3 = tr32(up ? 0.f : GV1(d1), up ? GV1(d1) : 0.f);
      float f = trd<XOR8>(tr16(e0, e2), tr16(e1, e3), lane & 8);
      f += dpp<XOR7>(f); f += dpp<XOR2>(f); f += dpp<XOR1>(f);
      if (c8 == 0) { lds[L_FIN + 32 + g8] = rbf(silu_f(rbf(f))); lds[L_FIN + 40 + g8] = xres; }
    }
    if (wave == 2 && blockIdx.x == 0) {
      wait_until<8>(tstart + T_GATE);
      unsigned dp = WS_DN + lane * 4 * 2048;
      FRESH(dp);
      u32x4 t0 = GET2(dp), t1 = GET2(dp + 2048), t2 = GET2(dp + 4096), t3 = GET2(dp + 6144);
      UNTIL_TAGGED(TAGOK(t0) && TAGOK(t1) && TAGOK(t2) && TAGOK(t3), dp,
                   (t0 = GET2(dp), t1 = GET2(dp + 2048), t2 = GET2(dp + 4096), t3 = GET2(dp + 6144)), 32);
      if (lane == 0) __hip_atomic_store((gu32*)(ws + WS_EPOCH), tag, RLX_AGENT);
    }
    __builtin_amdgcn_s_setprio(1);
  } else {
    const auto rv = RSRC(vc + kvbase, SK * DH * 2);
    u32x4 Kx[HX];
    __builtin_amdgcn_s_setprio(1);
    BAR();
#pragma unroll
    for (int g = NE; g < NG; ++g) R[g] = KV_LOAD(rk, HK + g);
#pragma unroll
    for (int i = 0; i < HX; ++i) Kx[i] = KV_LOAD(rk, i);
    BAR();
    const u32x4 qv = gather_q(lds, c);
    tag = __builtin_amdgcn_readfirstlane(reinterpret_cast<const unsigned*>(lds)[L_CNT + C_TAG]);
#define REFILL(n, NEXT) do { asm volatile("" :: "v"(R[(n) % NR])); SB(); R[((n) + NG) % NR] = NEXT; SB(); } while (0)
#define PACE_ROUND(n) \
      const int late = (int)((unsigned)(__builtin_amdgcn_s_memrealtime() - tstart) - (unsigned)(((n) * PACE) >> 9)) - line0; \
      if (late > 0) line0 += LEAK;
#define HOLD() do { if (late < 0) asm volatile("s_waitcnt vmcnt(%0)" :: "n"(NLO - 1)); \
                    else if (late < MIDW) asm volatile("s_waitcnt vmcnt(%0)" :: "n"(NMID - 1)); SB(); } while (0)
    int line0 = PACE0;
#define KSTEP(n, i, NEXT) do { REFILL(n, NEXT); pw[(i) * 4 + r] = score(R[(n) % NR], qv); SB(); } while (0)
    constexpr int KA = (NKC - NG) / NR * NR;
#pragma unroll
    for (int g = 0; g < NR; ++g) KSTEP(g, HK + g, KV_LOAD(rk, HK + g + NG));
#pragma unroll
    for (int i = 0; i < HX; ++i) pw[i * 4 + r] = score(Kx[i], qv);
    SB();
#pragma nounroll
    for (int i = NR; i < KA; i += NR) {
      PACE_ROUND(HK + i)
#pragma unroll
      for (int g = 0; g < NR; ++g) { HOLD(); KSTEP(g, HK + i + g, KV_LOAD(rk, HK + i + g + NG)); }
    }
#pragma unroll
    for (int i = KA; i < NKC; ++i) {
      if (i + NG < NKC) KSTEP(i, HK + i, KV_LOAD(rk, HK + i + NG));
      else KSTEP(i, HK + i, KV_LOAD(rv, i + NG - NKC));
    }
    float mb;
    {
      lds_wait<1>(lds, C_SC + sw, 1);
      f32x4 a[4];
      float mx = -INFINITY;
#pragma unroll
      for (int u = 0; u < 4; ++u) { a[u] = *reinterpret_cast<const f32x4*>(pw + u * 256 + lane * 4); mx = fmaxf(fmaxf(mx, fmaxf(a[u][0], a[u][1])), fmaxf(a[u][2], a[u][3])); }
      mb = wave_max(mx) * LOG2E;
      float ls = 0.f;
#pragma unroll
      for (int u = 0; u < 4; ++u) {
#pragma unroll
        for (int k = 0; k < 4; ++k) a[u][k] = __builtin_amdgcn_exp2f(fmaf(a[u][k], LOG2E, -mb));
        *reinterpret_cast<f32x4*>(pw + u * 256 + lane * 4) = a[u];
        ls += (a[u][0] + a[u][1]) + (a[u][2] + a[u][3]);
      }
      const float lw = wave_sum(ls);
      if (lane == 0) { lds[L_ML + sw] = mb; lds[L_ML + 4 + sw] = lw; }
      lds_arrive(lds, C_ML, lane);
    }
    SB();
    float acc[8];
#pragma unroll
    for (int k = 0; k < 8; ++k) acc[k] = 0.f;
#define VSTEP(n, j, MORE) do { \
      const float p_ = pw[(j) * 4 + r]; \
      if (MORE) REFILL(n, KV_LOAD(rv, (j) + NG)); \
      _Pragma("unroll") for (int k = 0; k < 4; ++k) { acc[2 * k] = fmaf(p_, lo16(R[(n) % NR][k]), acc[2 * k]); acc[2 * k + 1] = fmaf(p_, hi16(R[(n) % NR][k]), acc[2 * k + 1]); } \
      asm volatile("" : "+v"(acc[0]), "+v"(acc[1]), "+v"(acc[2]), "+v"(acc[3]), "+v"(acc[4]), "+v"(acc[5]), "+v"(acc[6]), "+v"(acc[7])); SB(); \
    } while (0)
    constexpr int VA = (NLD - NG) / NR * NR;
#pragma nounroll
    for (int j = 0; j < VA; j += NR) {
      PACE_ROUND(NLD + j)
#pragma unroll
      for (int g = 0; g < NR; ++g) { HOLD(); VSTEP(NKC + g, j + g, true); }
    }
#pragma unroll
    for (int j = VA; j < NLD - NG; ++j) VSTEP(NKC + j, j, true);
    lds_wait<1>(lds, C_WO + sw, 1);
#pragma unroll
    for (int i = 0; i < 16; ++i) Wo[i] = *reinterpret_cast<const u32x4*>(lds + L_WO + (sw * 16 + i) * 256 + lane * 4);
    lds_wait<1>(lds, C_NRM, 1);
    float sc = __builtin_amdgcn_exp2f(mb - lds[L_CNT + C_MW]) * lds[L_CNT + C_MW + 1];
    asm volatile("" : "+v"(sc)); SB();
#pragma unroll
    for (int j = NLD - NG; j < NLD; ++j) VSTEP(NKC + j, j, false);
    *reinterpret_cast<float2*>(lds + L_REC + sw * DH + 8 * c + 2 * r) =
        make_float2(sc * tr_rows(acc[0], acc[2], acc[4], acc[6]), sc * tr_rows(acc[1], acc[3], acc[5], acc[7]));
  }
  BAR();

  {
    const float2 v0 = *reinterpret_cast<const float2*>(lds + L_REC + 2 * lane), v1 = *reinterpret_cast<const float2*>(lds + L_REC + DH + 2 * lane);
    const float2 v2 = *reinterpret_cast<const float2*>(lds + L_REC + 2 * DH + 2 * lane), v3 = *reinterpret_cast<const float2*>(lds + L_REC + 3 * DH + 2 * lane);
    const float o0 = (v0.x + v1.x) + (v2.x + v3.x), o1 = (v0.y + v1.y) + (v2.y + v3.y);
    unsigned* ot = reinterpret_cast<unsigned*>(lds + L_OT + wave * DH);
    const unsigned oh = pk2bf(o0, o1);
    ot[lane] = oh;
    ot[64 + lane] = pk2bf(o0 - lo16(oh), o1 - hi16(oh));
    u32x4 ph[2], pl[2];
#pragma unroll
    for (int u = 0; u < 2; ++u) { ph[u] = *reinterpret_cast<const u32x4*>(ot + 8 * g8 + 4 * u); pl[u] = *reinterpret_cast<const u32x4*>(ot + 64 + 8 * g8 + 4 * u); }
    float a[8];
#pragma unroll
    for (int k = 0; k < 8; ++k) a[k] = 0.f;
#pragma unroll
    for (int j = 0; j < 8; ++j)
#pragma unroll
      for (int k = 0; k < 8; ++k) a[k] = dot2(Wo[2 * j + (k & 1)][k >> 1], ph[j >> 2][j & 3], dot2(Wo[2 * j + (k & 1)][k >> 1], pl[j >> 2][j & 3], a[k]));
    float y0 = tr_rows(a[0], a[2], a[4], a[6]), y1 = tr_rows(a[1], a[3], a[5], a[7]);
    y0 += dpp<XOR8>(y0); y1 += dpp<XOR8>(y1);
    if ((lane & 8) == 0) PUB2F(WS_Y + ((b * 64 + 8 * wave + c8) * 64 + slot) * 64 + r * 16, y0, y1);
  }

  if (wave < 4) {
    unsigned yp = WS_Y + (bc * 64 + cb) * 4096 + wave * 1024 + lane * 16;
    __builtin_amdgcn_s_sleep(14);
    FRESH(yp);
    u32x4 g = GET2(yp);
    UNTIL_TAGGED(TAGOK(g), yp, g = GET2(yp), 2);
    float y = trd<XOR8>(GV0(g), GV1(g), lane & 8);
    y += dpp<SHL4>(y);
    y = tr16(y, y);
    y = tr32(y, y);
    if (lane < 16 && (lane & 4) == 0) lds[L_FIN + wave * 8 + 2 * (lane & 3) + (lane >> 3)] = y;
    __builtin_amdgcn_fence(__ATOMIC_RELEASE, "workgroup");
    unsigned arr = 0;
    if (lane == 0) arr = __hip_atomic_fetch_add(reinterpret_cast<unsigned*>(lds) + L_CNT + C_FIN, 1u, RLX_WG);
    if (__builtin_amdgcn_readfirstlane(arr) == 3) {
      __builtin_amdgcn_fence(__ATOMIC_ACQUIRE, "workgroup");
      const float* f = lds + L_FIN + c8;
      const unsigned v = f2bf(f[40] + rbf(rbf((f[0] + f[8]) + (f[16] + f[24])) * f[32]));
      u32x4 o;
#pragma unroll
      for (int k = 0; k < 4; ++k) o[k] = __builtin_amdgcn_readlane(v, 2 * k) | (__builtin_amdgcn_readlane(v, 2 * k + 1) << 16);
      if (lane == 0) __builtin_amdgcn_raw_buffer_store_b128(o, RSRC(out, B * D * 2), (bc * D + 8 * cb) * 2, 0, SC1_AUX);
    }
  }
}

extern "C" void kernel_launch(void* const* d_in, const int* in_sizes, int n_in, void* d_out, int out_size, void* d_ws, size_t ws_size, hipStream_t stream) {
  fused_decode<<<NWG, NT, 0, stream>>>((const unsigned short*)d_in[0], (const unsigned short*)d_in[1], (const unsigned short*)d_in[2], (const unsigned short*)d_in[3],
                                       (const unsigned short*)d_in[4], (const unsigned short*)d_in[6], (const unsigned short*)d_in[7], (const unsigned short*)d_in[8],
                                       (unsigned short*)d_out, (unsigned char*)d_ws);
}
```
